```python
import torch

def _softmax(x, axis=-1):
    return torch.softmax(x, dim=axis)

def _gelu(x):
    return torch.nn.functional.gelu(x, approximate='tanh')

def _silu(x):
    return torch.nn.functional.silu(x)

def _sigmoid(x):
    return torch.sigmoid(x)

def _layernorm(x, axis=-1, eps=1e-05):
    m = x.mean(dim=axis, keepdim=True)
    v = x.var(dim=axis, keepdim=True, unbiased=False)
    return (x - m) / torch.sqrt(v + eps)

def _scatter_add(tgt, idx, src, axis=0):
    return tgt.clone().index_add_(axis, idx, src)

def _segment_sum(src, seg, n):
    out = torch.zeros((n,) + src.shape[1:], dtype=src.dtype)
    return out.index_add_(0, seg, src)

def _pad(x, pw):
    flat = tuple((v for pair in reversed(pw) for v in pair))
    return torch.nn.functional.pad(x, flat)
kernel_name = 'gated_mix__memory_aug_recurrent_mem_tr__linear_attn_block__prompt'

def setup_inputs():

    _w = lambda a: (lambda t: t.to(torch.bfloat16) if t.is_floating_point() else t)(torch.as_tensor(a))

    def _A_setup():
        B, S, Dm = (1, 2048, 576)
        H, Dh = (32, 128)
        M, seg = (8, 32)
        seg = min(seg, S)
        S = S // seg * seg
        return {'x': _w((lambda *s: torch.randn(*s, dtype=torch.bfloat16) * 0.05)(B, S, Dm)), 'mem0': _w((lambda *s: torch.randn(*s, dtype=torch.bfloat16) * 0.8)(B, M, Dm)), 'Wq': _w((lambda *s: torch.randn(*s, dtype=torch.bfloat16) * 3.2)(Dm, H, Dh) * Dm ** (-0.5)), 'Wk': _w((lambda *s: torch.randn(*s, dtype=torch.bfloat16) * 3.2)(Dm, H, Dh) * Dm ** (-0.5)), 'Wv': _w((lambda *s: torch.randn(*s, dtype=torch.bfloat16) * 0.8)(Dm, H, Dh) * Dm ** (-0.5)), 'Wo': _w((lambda *s: torch.randn(*s, dtype=torch.bfloat16) * 0.8)(H, Dh, Dm) * (H * Dh) ** (-0.5)), 'seg_len': seg}
    _A_in = _A_setup()

    def _B_setup():
        B, S, D = (1, 2048, 576)
        return {'x': _w((lambda *s: torch.randn(*s, dtype=torch.bfloat16) * 0.05)(B, S, D)), 'wqkv': _w((lambda *s: torch.randn(*s, dtype=torch.bfloat16) * 0.8)(D, 3 * D)), 'wo': _w((lambda *s: torch.randn(*s, dtype=torch.bfloat16) * 0.2)(D, D))}
    _B_in = _B_setup()
    return {'A_x': _A_in['x'], 'A_mem0': _A_in['mem0'], 'A_Wq': _A_in['Wq'], 'A_Wk': _A_in['Wk'], 'A_Wv': _A_in['Wv'], 'A_Wo': _A_in['Wo'], 'A_seg_len': _A_in['seg_len'], 'B_x': _B_in['x'], 'B_wqkv': _B_in['wqkv'], 'B_wo': _B_in['wo'], 'gate_w': _w((lambda *s: torch.randn(*s, dtype=torch.bfloat16) * 0.05)(576) * 16.0)}

def reference(A_x, A_mem0, A_Wq, A_Wk, A_Wv, A_Wo, A_seg_len, B_x, B_wqkv, B_wo, gate_w):

    def _A_ref(x, mem0, Wq, Wk, Wv, Wo, seg_len):
        B, S, Dm = x.shape
        H, Dh = (Wq.shape[1], Wq.shape[2])
        M = mem0.shape[1]
        scale = Dh ** (-0.5)
        n_seg = S // seg_len
        L = M + seg_len
        idx = torch.arange(L)
        mask = (idx[:, None] < M) | (idx[:, None] >= idx[None, :])
        mem = mem0
        outs = []
        for s in range(n_seg):
            xs = x[:, s * seg_len:(s + 1) * seg_len, :].clone()
            h = torch.concatenate([mem, xs], axis=1)
            q = torch.einsum('btd,dhe->bhte', h, Wq)
            k = torch.einsum('btd,dhe->bhte', h, Wk)
            v = torch.einsum('btd,dhe->bhte', h, Wv)
            scores = torch.einsum('bhqe,bhke->bhqk', q, k) * scale
            scores = torch.where(mask[None, None], scores, -1000000000.0)
            p = _softmax(scores, axis=-1)
            o = torch.einsum('bhqk,bhke->bhqe', p, v)
            o = torch.einsum('bhte,hed->btd', o, Wo)
            mem = o[:, :M, :].clone()
            outs.append(o[:, M:, :])
        return torch.concatenate(outs, axis=1)

    def _B_ref(x, wqkv, wo):
        B, S, D = x.shape
        H = 32
        Dh = D // H
        qkv = (lambda x, *d: x.permute(*d))((x @ wqkv).reshape(B, S, 3, H, Dh), 2, 0, 3, 1, 4)
        q, k, v = (qkv[0], qkv[1], qkv[2])
        phi = lambda t: torch.where(t > 0, t + 1, torch.exp(t)) + 1e-06
        pq, pk = (phi(q), phi(k))
        kv = torch.einsum('bhsd,bhse->bhde', pk, v)
        z = torch.einsum('bhsd->bhd', pk)
        num = torch.einsum('bhqd,bhde->bhqe', pq, kv)
        den = torch.einsum('bhqd,bhd->bhq', pq, z)[..., None] + 1e-06
        o = num / den
        return (lambda x, *d: x.permute(*d))(o, 0, 2, 1, 3).reshape(B, S, D) @ wo
    x = A_x
    g = _sigmoid((x * gate_w).sum(-1, keepdims=True))
    a = _A_ref(x, A_mem0, A_Wq, A_Wk, A_Wv, A_Wo, A_seg_len)
    b = _B_ref(x, B_wqkv, B_wo)
    return g * a + (1 - g) * b
```

```cpp
#include <hip/hip_runtime.h>
#include <hip/hip_bf16.h>
#include <stdint.h>

typedef unsigned u32;
typedef unsigned short u16;
typedef unsigned v4u __attribute__((ext_vector_type(4)));
typedef unsigned v2u __attribute__((ext_vector_type(2)));
typedef unsigned v8u __attribute__((ext_vector_type(8)));
typedef unsigned v16u __attribute__((ext_vector_type(16)));
typedef unsigned v32u __attribute__((ext_vector_type(32)));
typedef float f32x4 __attribute__((ext_vector_type(4)));
typedef float f32x2 __attribute__((ext_vector_type(2)));
typedef __bf16 bf16x2 __attribute__((ext_vector_type(2)));
typedef __bf16 bf16x8 __attribute__((ext_vector_type(8)));
typedef short s16x4 __attribute__((ext_vector_type(4)));
#define LAS __attribute__((address_space(3)))
#define GAS __attribute__((address_space(1)))
#define CHAIN_ASM \
  "s_lshl_b32 s19, s12, 8\n\t" \
  "s_add_u32 s19, s19, 0x5c00000\n\t" \
  "s_lshl_b32 s14, s12, 11\n\t" \
  "s_add_u32 s14, s14, s35\n\t" \
  "s_add_u32 s15, s14, 0x2400000\n\t" \
  "s_lshl_b32 s13, s14, 2\n\t" \
  "s_add_u32 s14, s14, 0x2000000\n\t" \
  "s_add_u32 s13, s13, 0x1000000\n\t" \
  "s_lshl_b32 s12, s12, 13\n\t" \
  "s_mov_b32 s20, 0x3e0293ee\n\t" \
  "s_mov_b32 s56, 0x3e0293ee\n\t" \
  "s_mov_b32 s57, 0x3e0293ee\n\t" \
  "v_mov_b32 v24, 0x3f803f80\n\t" \
  "v_mov_b32 v25, 0x3f803f80\n\t" \
  "v_mov_b32 v26, 0x3f803f80\n\t" \
  "v_mov_b32 v27, 0x3f803f80\n\t" \
  "s_mov_b32 s52, -1\n\t" \
  "s_mov_b32 s53, 0\n\t" \
  "s_mov_b32 s59, 0\n\t" \
  "v_cndmask_b32 v28, 0, v24, s[52:53]\n\t" \
  "v_cndmask_b32 v29, v24, 0, s[52:53]\n\t" \
  "s_mov_b32 s21, 0\n\t" \
  "s_mov_b32 s31, 0\n\t" \
  "s_mov_b32 s32, 0\n\t" \
  "s_mov_b32 s33, 0\n\t" \
  "s_mov_b32 s85, 0\n\t" \
  "s_mov_b32 s28, 0\n\t" \
  "s_memrealtime s[66:67]\n\t" \
  "s_memtime s[36:37]\n\t" \
  "s_waitcnt lgkmcnt(0)\n\t" \
  "s_mul_i32 s65, s66, 24\n\t" \
  "s_sub_u32 s65, s65, s36\n\t" \
  "s_mov_b32 s62, 12\n\t" \
  "CH_CAL0:\n\t" \
  "s_memrealtime s[66:67]\n\t" \
  "s_memtime s[36:37]\n\t" \
  "s_waitcnt lgkmcnt(0)\n\t" \
  "s_mul_i32 s34, s66, 24\n\t" \
  "s_sub_u32 s34, s34, s36\n\t" \
  "s_sub_u32 s34, s34, s65\n\t" \
  "s_max_i32 s34, s34, 0\n\t" \
  "s_add_u32 s65, s65, s34\n\t" \
  "s_sub_u32 s62, s62, 1\n\t" \
  "s_cmp_gt_i32 s62, 0\n\t" \
  "s_cbranch_scc1 CH_CAL0\n\t" \
  "s_mov_b32 s76, s65\n\t" \
  "s_mov_b32 s81, 200\n\t" \
  "s_mov_b32 s68, 0\n\t" \
  "s_mov_b32 s69, s74\n\t" \
  "s_add_u32 s26, s77, 0x140000\n\t" \
  "s_add_u32 s78, s77, 0x1e0000\n\t" \
  "s_add_u32 s79, s77, 0x200000\n\t" \
  "s_add_u32 s34, s12, 4096\n\t" \
  "buffer_load_dwordx4 v[32:35], v1, s[8:11], s12 offen sc1\n\t" \
  "buffer_load_dwordx4 v[36:39], v1, s[8:11], s12 offen offset:1024 sc1\n\t" \
  "buffer_load_dwordx4 v[40:43], v1, s[8:11], s12 offen offset:2048 sc1\n\t" \
  "buffer_load_dwordx4 v[44:47], v1, s[8:11], s12 offen offset:3072 sc1\n\t" \
  "buffer_load_dwordx4 v[48:51], v1, s[8:11], s34 offen sc1\n\t" \
  "buffer_load_dwordx4 v[52:55], v1, s[8:11], s34 offen offset:1024 sc1\n\t" \
  "buffer_load_dwordx4 v[56:59], v1, s[8:11], s34 offen offset:2048 sc1\n\t" \
  "buffer_load_dwordx4 v[60:63], v1, s[8:11], s34 offen offset:3072 sc1\n\t" \
  "buffer_load_dwordx4 v[64:67], v1, s[8:11], s13 offen sc1\n\t" \
  "buffer_load_dwordx4 v[68:71], v1, s[8:11], s13 offen offset:1024 sc1\n\t" \
  "s_waitcnt vmcnt(0)\n\t" \
  "s_add_u32 s34, s21, 0\n\t" \
  "s_lshl_b32 s22, s34, 15\n\t" \
  "s_add_u32 s22, s22, s79\n\t" \
  "s_add_u32 s34, s34, 1\n\t" \
  "s_min_u32 s34, s34, 63\n\t" \
  "s_lshl_b32 s34, s34, 18\n\t" \
  "s_add_u32 s23, s12, s34\n\t" \
  "s_lshl_b32 s62, s35, 2\n\t" \
  "s_add_u32 s23, s23, s62\n\t" \
  "s_add_u32 s25, s13, s34\n\t" \
  "s_mov_b32 s27, s34\n\t" \
  "s_sub_u32 s34, s19, s34\n\t" \
  "v_writelane_b32 v13, s34, 32\n\t" \
  "s_mov_b32 s84, s71\n\t" \
  "s_mov_b32 s71, s26\n\t" \
  "s_add_u32 s26, s26, 0xa0000\n\t" \
  "s_cmp_eq_u32 s26, s78\n\t" \
  "s_cselect_b32 s26, s77, s26\n\t" \
  "s_add_u32 s54, s26, 0x4000\n\t" \
  "s_add_u32 s55, s26, 0x8000\n\t" \
  "ds_read_b128 v[132:135], v2 offset:0\n\t" \
  "ds_read_b128 v[136:139], v2 offset:1024\n\t" \
  "ds_read_b128 v[140:143], v2 offset:2048\n\t" \
  "ds_read_b128 v[144:147], v2 offset:3072\n\t" \
  "ds_read_b128 v[148:151], v2 offset:4096\n\t" \
  "ds_read_b128 v[152:155], v2 offset:5120\n\t" \
  "ds_read_b128 v[156:159], v2 offset:6144\n\t" \
  "ds_read_b128 v[160:163], v2 offset:7168\n\t" \
  "ds_read_b128 v[164:167], v2 offset:8192\n\t" \
  "ds_read_b128 v[168:171], v2 offset:9216\n\t" \
  "v_mov_b64 v[72:73], 0\n\t" \
  "v_mov_b64 v[74:75], 0\n\t" \
  "v_mov_b64 v[76:77], 0\n\t" \
  "v_mov_b64 v[78:79], 0\n\t" \
  "v_mov_b64 v[80:81], 0\n\t" \
  "v_mov_b64 v[82:83], 0\n\t" \
  "v_mov_b64 v[84:85], 0\n\t" \
  "v_mov_b64 v[86:87], 0\n\t" \
  "v_mov_b64 v[88:89], 0\n\t" \
  "v_mov_b64 v[90:91], 0\n\t" \
  "v_mov_b64 v[92:93], 0\n\t" \
  "v_mov_b64 v[94:95], 0\n\t" \
  "s_waitcnt lgkmcnt(0)\n\t" \
  "CH_TOP:\n\t" \
  "s_nop 1\n\t" \
  "s_mov_b32 s30, 0\n\t" \
  "s_cmp_eq_u32 s21, 0\n\t" \
  "s_cbranch_scc1 CH_A\n\t" \
  "s_add_i32 s65, s76, s81\n\t" \
  "s_mov_b32 s29, s58\n\t" \
  "s_waitcnt lgkmcnt(0)\n\t" \
  "s_barrier\n\t" \
  "s_sub_u32 s29, s29, s65\n\t" \
  "s_sub_u32 s29, s29, 40\n\t" \
  "CH_W2:\n\t" \
  "s_memtime s[36:37]\n\t" \
  "s_waitcnt lgkmcnt(0)\n\t" \
  "s_sub_u32 s62, s29, s36\n\t" \
  "s_sub_u32 s34, s62, 121\n\t" \
  "s_cmp_lt_u32 s34, 0x8000\n\t" \
  "s_cbranch_scc1 CH_W2\n\t" \
  "s_cmp_lt_i32 s62, 64\n\t" \
  "s_cbranch_scc1 CH_W264\n\t" \
  "s_sub_u32 s62, s62, 64\n\t" \
  "s_nop 13\n\t" \
  "CH_W264:\n\t" \
  "s_cmp_lt_i32 s62, 32\n\t" \
  "s_cbranch_scc1 CH_W232\n\t" \
  "s_sub_u32 s62, s62, 32\n\t" \
  "s_nop 5\n\t" \
  "CH_W232:\n\t" \
  "s_cmp_lt_i32 s62, 16\n\t" \
  "s_cbranch_scc1 CH_W216\n\t" \
  "s_sub_u32 s62, s62, 16\n\t" \
  "s_nop 1\n\t" \
  "CH_W216:\n\t" \
  "CH_R2:\n\t" \
  "buffer_load_dwordx4 v[96:99], v10, s[8:11], s22 offen sc1\n\t" \
  "buffer_load_dwordx4 v[100:103], v11, s[8:11], s22 offen sc1\n\t" \
  "buffer_load_dwordx4 v[104:107], v12, s[8:11], s22 offen sc1\n\t" \
  "s_waitcnt vmcnt(2)\n\t" \
  "ds_write_b128 v8, v[96:99] offset:0\n\t" \
  "v_max3_u32 v108, v96, v97, v98\n\t" \
  "s_waitcnt vmcnt(1)\n\t" \
  "ds_write_b128 v8, v[100:103] offset:1024\n\t" \
  "v_max3_u32 v108, v108, v99, v100\n\t" \
  "v_max3_u32 v109, v101, v102, v103\n\t" \
  "s_waitcnt vmcnt(0)\n\t" \
  "ds_write_b128 v9, v[104:107] offset:0\n\t" \
  "v_max3_u32 v108, v108, v104, v105\n\t" \
  "v_max3_u32 v109, v109, v106, v107\n\t" \
  "v_max_u32 v108, v108, v109\n\t" \
  "v_cmp_eq_u32 vcc, -1, v108\n\t" \
  "s_cbranch_vccnz CH_F2\n\t" \
  "CH_A:\n\t" \
  "s_waitcnt lgkmcnt(0)\n\t" \
  "s_barrier\n\t" \
  "ds_read_b128 v[96:99], v1 offset:0\n\t" \
  "ds_read_b128 v[100:103], v1 offset:1024\n\t" \
  "ds_read_b128 v[104:107], v1 offset:2048\n\t" \
  "s_waitcnt lgkmcnt(2)\n\t" \
  "v_smfmac_f32_16x16x64_bf16 v[80:83], v[96:99], a[0:7], v0\n\t" \
  "ds_read_b128 v[108:111], v1 offset:3072\n\t" \
  "v_smfmac_f32_16x16x64_bf16 v[84:87], v[96:99], a[8:15], v0\n\t" \
  "ds_read_b128 v[112:115], v1 offset:4096\n\t" \
  "v_smfmac_f32_16x16x64_bf16 v[88:91], v[96:99], a[16:23], v0\n\t" \
  "ds_read_b128 v[116:119], v1 offset:5120\n\t" \
  "v_smfmac_f32_16x16x64_bf16 v[92:95], v[96:99], a[24:31], v0\n\t" \
  "ds_read_b128 v[120:123], v1 offset:6144\n\t" \
  "s_waitcnt lgkmcnt(5)\n\t" \
  "v_smfmac_f32_16x16x64_bf16 v[80:83], v[100:103], a[32:39], v0\n\t" \
  "ds_read_b128 v[124:127], v1 offset:7168\n\t" \
  "v_smfmac_f32_16x16x64_bf16 v[84:87], v[100:103], a[40:47], v0\n\t" \
  "ds_read_b128 v[128:131], v1 offset:8192\n\t" \
  "v_smfmac_f32_16x16x64_bf16 v[88:91], v[100:103], a[48:55], v0\n\t" \
  "ds_read_b32 v173, v1 offset:15360\n\t" \
  "v_smfmac_f32_16x16x64_bf16 v[92:95], v[100:103], a[56:63], v0\n\t" \
  "ds_read_b32 v30, v1 offset:15364\n\t" \
  "s_waitcnt lgkmcnt(8)\n\t" \
  "v_smfmac_f32_16x16x64_bf16 v[80:83], v[104:107], a[64:71], v0\n\t" \
  "ds_read_b64 v[174:175], v1 offset:9216\n\t" \
  "v_smfmac_f32_16x16x64_bf16 v[84:87], v[104:107], a[72:79], v0\n\t" \
  "buffer_load_dword v172, v13, s[8:11], s27 offen sc1\n\t" \
  "v_smfmac_f32_16x16x64_bf16 v[88:91], v[104:107], a[80:87], v0\n\t" \
  "v_smfmac_f32_16x16x64_bf16 v[92:95], v[104:107], a[88:95], v0\n\t" \
  "s_waitcnt lgkmcnt(1)\n\t" \
  "v_subrev_u32 v173, s58, v173\n\t" \
  "v_subrev_u32 v30, s58, v30\n\t" \
  "v_smfmac_f32_16x16x64_bf16 v[80:83], v[108:111], a[96:103], v0\n\t" \
  "s_waitcnt lgkmcnt(0)\n\t" \
  "v_readfirstlane_b32 s82, v174\n\t" \
  "v_smfmac_f32_16x16x64_bf16 v[84:87], v[108:111], a[104:111], v0\n\t" \
  "v_readfirstlane_b32 s83, v175\n\t" \
  "v_smfmac_f32_16x16x64_bf16 v[88:91], v[108:111], a[112:119], v0\n\t" \
  "v_max_i32_dpp v173, v173, v173 row_ror:8 row_mask:0xf bank_mask:0xf\n\t" \
  "v_max_i32_dpp v30, v30, v30 row_ror:8 row_mask:0xf bank_mask:0xf\n\t" \
  "v_smfmac_f32_16x16x64_bf16 v[92:95], v[108:111], a[120:127], v0\n\t" \
  "v_smfmac_f32_16x16x64_bf16 v[72:75], v[108:111], v[132:139], v0\n\t" \
  "v_smfmac_f32_16x16x64_bf16 v[76:79], v[108:111], v[140:147], v0\n\t" \
  "v_smfmac_f32_16x16x64_bf16 v[80:83], v[112:115], a[128:135], v0\n\t" \
  "ds_read_b128 v[132:135], v2 offset:10240\n\t" \
  "v_max_i32_dpp v173, v173, v173 row_ror:4 row_mask:0xf bank_mask:0xf\n\t" \
  "v_max_i32_dpp v30, v30, v30 row_ror:4 row_mask:0xf bank_mask:0xf\n\t" \
  "v_smfmac_f32_16x16x64_bf16 v[84:87], v[112:115], a[136:143], v0\n\t" \
  "ds_read_b128 v[136:139], v2 offset:11264\n\t" \
  "v_smfmac_f32_16x16x64_bf16 v[88:91], v[112:115], a[144:151], v0\n\t" \
  "ds_read_b128 v[140:143], v2 offset:12288\n\t" \
  "v_smfmac_f32_16x16x64_bf16 v[92:95], v[112:115], a[152:159], v0\n\t" \
  "ds_read_b128 v[144:147], v2 offset:13312\n\t" \
  "v_smfmac_f32_16x16x64_bf16 v[72:75], v[112:115], v[148:155], v0\n\t" \
  "v_max_i32_dpp v173, v173, v173 row_ror:2 row_mask:0xf bank_mask:0xf\n\t" \
  "v_max_i32_dpp v30, v30, v30 row_ror:2 row_mask:0xf bank_mask:0xf\n\t" \
  "v_smfmac_f32_16x16x64_bf16 v[76:79], v[112:115], v[156:163], v0\n\t" \
  "v_smfmac_f32_16x16x64_bf16 v[80:83], v[116:119], a[160:167], v0\n\t" \
  "ds_read_b128 v[148:151], v2 offset:14336\n\t" \
  "v_smfmac_f32_16x16x64_bf16 v[84:87], v[116:119], a[168:175], v0\n\t" \
  "ds_read_b128 v[152:155], v2 offset:15360\n\t" \
  "v_max_i32_dpp v173, v173, v173 row_ror:1 row_mask:0xf bank_mask:0xf\n\t" \
  "v_max_i32_dpp v30, v30, v30 row_ror:1 row_mask:0xf bank_mask:0xf\n\t" \
  "v_smfmac_f32_16x16x64_bf16 v[88:91], v[116:119], a[176:183], v0\n\t" \
  "ds_read_b128 v[156:159], v2 offset:16384\n\t" \
  "v_mov_b32 v174, v173\n\t" \
  "v_mov_b32 v31, v30\n\t" \
  "v_smfmac_f32_16x16x64_bf16 v[92:95], v[116:119], a[184:191], v0\n\t" \
  "ds_read_b128 v[160:163], v2 offset:17408\n\t" \
  "v_smfmac_f32_16x16x64_bf16 v[72:75], v[116:119], v[164:171], v0\n\t" \
  "v_smfmac_f32_16x16x64_bf16 v[80:83], v[120:123], a[192:199], v0\n\t" \
  "ds_read_b128 v[164:167], v2 offset:18432\n\t" \
  "v_permlane16_swap_b32 v173, v174\n\t" \
  "v_smfmac_f32_16x16x64_bf16 v[84:87], v[120:123], a[200:207], v0\n\t" \
  "ds_read_b128 v[168:171], v2 offset:19456\n\t" \
  "v_permlane16_swap_b32 v30, v31\n\t" \
  "v_smfmac_f32_16x16x64_bf16 v[88:91], v[120:123], a[208:215], v0\n\t" \
  "v_max_i32 v173, v173, v174\n\t" \
  "v_max_i32 v175, v30, v31\n\t" \
  "v_smfmac_f32_16x16x64_bf16 v[92:95], v[120:123], a[216:223], v0\n\t" \
  "s_waitcnt lgkmcnt(8)\n\t" \
  "v_smfmac_f32_16x16x64_bf16 v[76:79], v[116:119], v[132:139], v0\n\t" \
  "s_waitcnt lgkmcnt(6)\n\t" \
  "v_smfmac_f32_16x16x64_bf16 v[72:75], v[120:123], v[140:147], v0\n\t" \
  "v_smfmac_f32_16x16x64_bf16 v[80:83], v[124:127], a[224:231], v0\n\t" \
  "ds_read_b128 v[132:135], v2 offset:20480\n\t" \
  "v_permlane32_swap_b32 v173, v175\n\t" \
  "v_smfmac_f32_16x16x64_bf16 v[84:87], v[124:127], a[232:239], v0\n\t" \
  "ds_read_b128 v[136:139], v2 offset:21504\n\t" \
  "v_max_i32 v173, v173, v175\n\t" \
  "v_smfmac_f32_16x16x64_bf16 v[88:91], v[124:127], a[240:247], v0\n\t" \
  "ds_read_b128 v[140:143], v2 offset:22528\n\t" \
  "v_smfmac_f32_16x16x64_bf16 v[92:95], v[124:127], a[248:255], v0\n\t" \
  "ds_read_b128 v[144:147], v2 offset:23552\n\t" \
  "v_readlane_b32 s60, v173, 0\n\t" \
  "v_readlane_b32 s70, v173, 32\n\t" \
  "v_smfmac_f32_16x16x64_bf16 v[80:83], v[128:131], v[176:183], v0\n\t" \
  "v_smfmac_f32_16x16x64_bf16 v[84:87], v[128:131], v[184:191], v0\n\t" \
  "v_smfmac_f32_16x16x64_bf16 v[88:91], v[128:131], v[192:199], v0\n\t" \
  "v_smfmac_f32_16x16x64_bf16 v[92:95], v[128:131], v[200:207], v0\n\t" \
  "v_smfmac_f32_16x16x64_bf16 v[72:75], v[96:99], v[208:215], v0\n\t" \
  "v_smfmac_f32_16x16x64_bf16 v[76:79], v[96:99], v[216:223], v0\n\t" \
  "s_nop 2\n\t" \
  "v_add_f32 v80, v80, v81\n\t" \
  "v_add_f32 v82, v82, v83\n\t" \
  "v_cvt_pk_bf16_f32 v30, v80, v82\n\t" \
  "v_add_f32 v84, v84, v85\n\t" \
  "v_add_f32 v86, v86, v87\n\t" \
  "v_cvt_pk_bf16_f32 v31, v84, v86\n\t" \
  "ds_write2st64_b32 v3, v30, v31 offset0:44 offset1:45\n\t" \
  "v_add_f32 v88, v88, v89\n\t" \
  "v_add_f32 v90, v90, v91\n\t" \
  "v_cvt_pk_bf16_f32 v174, v88, v90\n\t" \
  "v_add_f32 v92, v92, v93\n\t" \
  "v_add_f32 v94, v94, v95\n\t" \
  "v_cvt_pk_bf16_f32 v175, v92, v94\n\t" \
  "ds_write2st64_b32 v3, v174, v175 offset0:46 offset1:47\n\t" \
  "s_waitcnt lgkmcnt(10)\n\t" \
  "v_smfmac_f32_16x16x64_bf16 v[76:79], v[120:123], v[148:155], v0\n\t" \
  "s_waitcnt lgkmcnt(8)\n\t" \
  "v_smfmac_f32_16x16x64_bf16 v[72:75], v[124:127], v[156:163], v0\n\t" \
  "s_waitcnt lgkmcnt(2)\n\t" \
  "v_smfmac_f32_16x16x64_bf16 v[76:79], v[128:131], v[140:147], v0\n\t" \
  "v_smfmac_f32_16x16x64_bf16 v[72:75], v[100:103], v[224:231], v0\n\t" \
  "s_waitcnt lgkmcnt(0)\n\t" \
  "s_barrier\n\t" \
  "ds_read_b64_tr_b16 v[80:81], v4 offset:11264\n\t" \
  "ds_read_b64_tr_b16 v[82:83], v4 offset:11520\n\t" \
  "ds_read_b64_tr_b16 v[84:85], v4 offset:13312\n\t" \
  "ds_read_b64_tr_b16 v[86:87], v4 offset:13568\n\t" \
  "v_smfmac_f32_16x16x64_bf16 v[76:79], v[100:103], v[232:239], v0\n\t" \
  "ds_read_b64_tr_b16 v[88:89], v4 offset:11776\n\t" \
  "ds_read_b64_tr_b16 v[90:91], v4 offset:12032\n\t" \
  "ds_read_b64_tr_b16 v[92:93], v4 offset:13824\n\t" \
  "ds_read_b64_tr_b16 v[94:95], v4 offset:14080\n\t" \
  "v_smfmac_f32_16x16x64_bf16 v[72:75], v[104:107], v[240:247], v0\n\t" \
  "ds_read_b64_tr_b16 v[140:141], v4 offset:12288\n\t" \
  "ds_read_b64_tr_b16 v[142:143], v4 offset:12544\n\t" \
  "ds_read_b64_tr_b16 v[144:145], v4 offset:14336\n\t" \
  "ds_read_b64_tr_b16 v[146:147], v4 offset:14592\n\t" \
  "v_smfmac_f32_16x16x64_bf16 v[76:79], v[104:107], v[248:255], v0\n\t" \
  "ds_read_b64_tr_b16 v[148:149], v4 offset:12800\n\t" \
  "ds_read_b64_tr_b16 v[150:151], v4 offset:13056\n\t" \
  "v_smfmac_f32_16x16x64_bf16 v[76:79], v[124:127], v[164:171], v0\n\t" \
  "s_waitcnt lgkmcnt(12)\n\t" \
  "v_mfma_f32_16x16x32_bf16 v[156:159], v[32:35], v[80:83], 0\n\t" \
  "ds_read_b64_tr_b16 v[152:153], v4 offset:14848\n\t" \
  "ds_read_b64_tr_b16 v[154:155], v4 offset:15104\n\t" \
  "v_mfma_f32_16x16x32_bf16 v[160:163], v[48:51], v[80:83], 0\n\t" \
  "v_smfmac_f32_16x16x64_bf16 v[72:75], v[128:131], v[132:139], v0\n\t" \
  "s_waitcnt lgkmcnt(12)\n\t" \
  "v_mfma_f32_16x16x32_bf16 v[96:99], v[84:87], v[80:83], 0\n\t" \
  "ds_read_b128 v[164:167], v2 offset:24576\n\t" \
  "ds_read_b128 v[168:171], v2 offset:25600\n\t" \
  "s_waitcnt lgkmcnt(12)\n\t" \
  "v_mfma_f32_16x16x32_bf16 v[156:159], v[36:39], v[88:91], v[156:159]\n\t" \
  "v_mfma_f32_16x16x32_bf16 v[160:163], v[52:55], v[88:91], v[160:163]\n\t" \
  "s_waitcnt lgkmcnt(10)\n\t" \
  "v_mfma_f32_16x16x32_bf16 v[96:99], v[92:95], v[88:91], v[96:99]\n\t" \
  "ds_read_b128 v[132:135], v2 offset:26624\n\t" \
  "ds_read_b128 v[136:139], v2 offset:27648\n\t" \
  "s_waitcnt lgkmcnt(10)\n\t" \
  "v_mfma_f32_16x16x32_bf16 v[156:159], v[40:43], v[140:143], v[156:159]\n\t" \
  "v_add_f32 v76, v76, v77\n\t" \
  "v_add_f32 v78, v78, v79\n\t" \
  "v_mfma_f32_16x16x32_bf16 v[160:163], v[56:59], v[140:143], v[160:163]\n\t" \
  "v_cvt_pk_bf16_f32 v117, v76, v78\n\t" \
  "v_add_f32 v72, v72, v73\n\t" \
  "s_waitcnt lgkmcnt(8)\n\t" \
  "v_mfma_f32_16x16x32_bf16 v[96:99], v[144:147], v[140:143], v[96:99]\n\t" \
  "v_add_f32 v74, v74, v75\n\t" \
  "v_cndmask_b32 v122, 0, v117, s[52:53]\n\t" \
  "s_waitcnt lgkmcnt(6)\n\t" \
  "v_mfma_f32_16x16x32_bf16 v[156:159], v[44:47], v[148:151], v[156:159]\n\t" \
  "v_cvt_pk_bf16_f32 v116, v72, v74\n\t" \
  "v_cndmask_b32 v123, v117, 0, s[52:53]\n\t" \
  "v_mfma_f32_16x16x32_bf16 v[160:163], v[60:63], v[148:151], v[160:163]\n\t" \
  "v_cndmask_b32 v120, 0, v116, s[52:53]\n\t" \
  "v_cndmask_b32 v121, v116, 0, s[52:53]\n\t" \
  "s_waitcnt lgkmcnt(4)\n\t" \
  "v_mfma_f32_16x16x32_bf16 v[96:99], v[152:155], v[148:151], v[96:99]\n\t" \
  "ds_read_b128 v[80:83], v2 offset:28672\n\t" \
  "ds_read_b128 v[84:87], v2 offset:29696\n\t" \
  "v_mov_b64 v[88:89], 0\n\t" \
  "v_mov_b64 v[90:91], 0\n\t" \
  "v_max3_f32 v30, v156, v157, v158\n\t" \
  "v_max3_f32 v31, v160, v161, v162\n\t" \
  "s_nop 1\n\t" \
  "v_cndmask_b32 v96, v98, v96, s[52:53]\n\t" \
  "v_cndmask_b32 v97, v99, v97, s[52:53]\n\t" \
  "v_max3_f32 v30, v30, v159, v96\n\t" \
  "v_max3_f32 v31, v31, v163, v97\n\t" \
  "v_max_f32 v174, v30, v31\n\t" \
  "v_max_f32 v175, v30, v31\n\t" \
  "ds_read_b128 v[32:35], v2 offset:30720\n\t" \
  "ds_read_b128 v[36:39], v2 offset:31744\n\t" \
  "v_permlane16_swap_b32 v174, v175\n\t" \
  "v_max_f32 v30, v174, v175\n\t" \
  "v_max_f32 v31, v174, v175\n\t" \
  "ds_read_b128 v[48:51], v2 offset:32768\n\t" \
  "ds_read_b128 v[52:55], v2 offset:33792\n\t" \
  "v_permlane32_swap_b32 v30, v31\n\t" \
  "v_max_f32 v30, v30, v31\n\t" \
  "v_mul_f32 v174, s20, v30\n\t" \
  "v_pk_fma_f32 v[96:97], v[96:97], s[56:57], v[174:175] op_sel_hi:[1,1,0] neg_lo:[0,0,1] neg_hi:[0,0,1]\n\t" \
  "v_pk_fma_f32 v[156:157], v[156:157], s[56:57], v[174:175] op_sel_hi:[1,1,0] neg_lo:[0,0,1] neg_hi:[0,0,1]\n\t" \
  "v_exp_f32 v96, v96\n\t" \
  "v_exp_f32 v97, v97\n\t" \
  "v_pk_fma_f32 v[158:159], v[158:159], s[56:57], v[174:175] op_sel_hi:[1,1,0] neg_lo:[0,0,1] neg_hi:[0,0,1]\n\t" \
  "v_exp_f32 v156, v156\n\t" \
  "v_exp_f32 v157, v157\n\t" \
  "v_cvt_pk_bf16_f32 v112, v96, v97\n\t" \
  "v_cvt_pk_bf16_f32 v113, v96, v97\n\t" \
  "v_pk_fma_f32 v[160:161], v[160:161], s[56:57], v[174:175] op_sel_hi:[1,1,0] neg_lo:[0,0,1] neg_hi:[0,0,1]\n\t" \
  "v_exp_f32 v158, v158\n\t" \
  "v_exp_f32 v159, v159\n\t" \
  "v_mfma_f32_16x16x16_bf16 v[100:103], v[120:121], v[112:113], 0\n\t" \
  "v_cvt_pk_bf16_f32 v108, v156, v157\n\t" \
  "v_pk_fma_f32 v[162:163], v[162:163], s[56:57], v[174:175] op_sel_hi:[1,1,0] neg_lo:[0,0,1] neg_hi:[0,0,1]\n\t" \
  "v_exp_f32 v160, v160\n\t" \
  "v_mfma_f32_16x16x16_bf16 v[104:107], v[122:123], v[112:113], 0\n\t" \
  "v_exp_f32 v161, v161\n\t" \
  "v_cvt_pk_bf16_f32 v109, v158, v159\n\t" \
  "ds_read_b128 v[140:143], v2 offset:34816\n\t" \
  "v_exp_f32 v162, v162\n\t" \
  "v_mfma_f32_16x16x16_bf16 v[148:151], v[112:113], v[28:29], 0\n\t" \
  "v_exp_f32 v163, v163\n\t" \
  "v_cvt_pk_bf16_f32 v110, v160, v161\n\t" \
  "ds_read_b128 v[144:147], v2 offset:35840\n\t" \
  "v_cvt_pk_bf16_f32 v111, v162, v163\n\t" \
  "v_mov_b64 v[40:41], 0\n\t" \
  "s_nop 0\n\t" \
  "v_mfma_f32_16x16x32_bf16 v[100:103], v[64:67], v[108:111], v[100:103]\n\t" \
  "v_mfma_f32_16x16x32_bf16 v[104:107], v[68:71], v[108:111], v[104:107]\n\t" \
  "v_mov_b64 v[42:43], 0\n\t" \
  "v_mov_b64 v[44:45], 0\n\t" \
  "v_mfma_f32_16x16x32_bf16 v[148:151], v[108:111], v[24:27], v[148:151]\n\t" \
  "v_mov_b64 v[46:47], 0\n\t" \
  "s_nop 2\n\t" \
  "v_cvt_pk_bf16_f32 v92, v100, v101\n\t" \
  "v_cvt_pk_bf16_f32 v93, v102, v103\n\t" \
  "v_cvt_pk_bf16_f32 v94, v104, v105\n\t" \
  "v_cvt_pk_bf16_f32 v95, v106, v107\n\t" \
  "ds_write_b128 v5, v[92:95] offset:0\n\t" \
  "v_mov_b32 v118, v0\n\t" \
  "s_nop 1\n\t" \
  "v_cndmask_b32 v148, v150, v148, s[52:53]\n\t" \
  "v_cndmask_b32 v149, v151, v149, s[52:53]\n\t" \
  "v_rcp_f32 v148, v148\n\t" \
  "v_rcp_f32 v149, v149\n\t" \
  "v_mov_b32 v118, v0\n\t" \
  "s_nop 1\n\t" \
  "v_mov_b32 v118, v0\n\t" \
  "s_nop 1\n\t" \
  "s_cmp_eq_u32 s21, 63\n\t" \
  "s_waitcnt lgkmcnt(0)\n\t" \
  "s_barrier\n\t" \
  "s_cbranch_scc1 CH_LAST\n\t" \
  "ds_read_b128 v[56:59], v1 offset:9216\n\t" \
  "ds_read_b128 v[60:63], v1 offset:10240\n\t" \
  "s_waitcnt vmcnt(0)\n\t" \
  "v_readlane_b32 s34, v172, 32\n\t" \
  "s_cmp_gt_i32 s34, s21\n\t" \
  "s_cbranch_scc0 CH_FK\n\t" \
  "CH_KOK:\n\t" \
  "buffer_load_dwordx4 v[124:127], v1, s[8:11], s23 offen sc1\n\t" \
  "buffer_load_dwordx4 v[128:131], v1, s[8:11], s23 offen offset:1024 sc1\n\t" \
  "s_waitcnt lgkmcnt(1)\n\t" \
  "v_smfmac_f32_16x16x64_bf16 v[88:91], v[56:59], v[164:171], v0\n\t" \
  "s_waitcnt lgkmcnt(0)\n\t" \
  "v_smfmac_f32_16x16x64_bf16 v[88:91], v[60:63], v[32:39], v0\n\t" \
  "v_smfmac_f32_16x16x64_bf16 v[40:43], v[56:59], v[132:139], v0\n\t" \
  "v_smfmac_f32_16x16x64_bf16 v[40:43], v[60:63], v[48:55], v0\n\t" \
  "v_smfmac_f32_16x16x64_bf16 v[44:47], v[56:59], v[80:87], v0\n\t" \
  "v_smfmac_f32_16x16x64_bf16 v[44:47], v[60:63], v[140:147], v0\n\t" \
  "s_nop 3\n\t" \
  "v_add_f32 v88, v88, v89\n\t" \
  "v_add_f32 v89, v90, v91\n\t" \
  "v_pk_mul_f32 v[88:89], v[88:89], v[148:149]\n\t" \
  "v_cvt_pk_bf16_f32 v30, v88, v89\n\t" \
  "v_min_u32 v30, -2, v30\n\t" \
  "buffer_store_dword v30, v6, s[8:11], s26 offen sc1\n\t" \
  "v_add_f32 v40, v40, v41\n\t" \
  "v_add_f32 v41, v42, v43\n\t" \
  "v_pk_mul_f32 v[40:41], v[40:41], v[148:149]\n\t" \
  "v_cvt_pk_bf16_f32 v31, v40, v41\n\t" \
  "v_min_u32 v31, -2, v31\n\t" \
  "buffer_store_dword v31, v6, s[8:11], s54 offen sc1\n\t" \
  "v_add_f32 v44, v44, v45\n\t" \
  "v_add_f32 v45, v46, v47\n\t" \
  "v_pk_mul_f32 v[44:45], v[44:45], v[148:149]\n\t" \
  "v_cvt_pk_bf16_f32 v174, v44, v45\n\t" \
  "v_min_u32 v174, -2, v174\n\t" \
  "buffer_store_dword v174, v6, s[8:11], s55 offen sc1\n\t" \
  "s_memtime s[36:37]\n\t" \
  "buffer_load_dwordx4 v[64:67], v1, s[8:11], s25 offen sc1\n\t" \
  "buffer_load_dwordx4 v[68:71], v1, s[8:11], s25 offen offset:1024 sc1\n\t" \
  "s_waitcnt vmcnt(5)\n\t" \
  "ds_write_b128 v7, v[124:127] offset:0\n\t" \
  "ds_write_b128 v7, v[128:131] offset:1024\n\t" \
  "s_mov_b32 s59, 0\n\t" \
  "s_cmp_eq_u32 s21, 0\n\t" \
  "s_cbranch_scc1 CH_NOCTL\n\t" \
  "s_mov_b32 s76, s82\n\t" \
  "s_mov_b32 s81, s83\n\t" \
  "s_mul_i32 s65, s81, 13\n\t" \
  "s_ashr_i32 s65, s65, 4\n\t" \
  "s_add_i32 s65, s65, s76\n\t" \
  "s_add_i32 s62, s60, s58\n\t" \
  "s_sub_i32 s34, s62, s61\n\t" \
  "s_add_i32 s70, s70, s58\n\t" \
  "s_sub_i32 s70, s70, s61\n\t" \
  "s_add_i32 s60, s60, s39\n\t" \
  "s_add_i32 s62, s62, s39\n\t" \
  "s_add_i32 s62, s62, 600\n\t" \
  "s_cmp_eq_u32 s21, 1\n\t" \
  "s_cselect_b32 s60, 0x1000, s60\n\t" \
  "s_cmp_gt_i32 s60, 200\n\t" \
  "s_cselect_b32 s61, s62, s58\n\t" \
  "s_cselect_b32 s59, 100, 0\n\t" \
  "s_mov_b32 s62, s68\n\t" \
  "s_cselect_b32 s68, 0, 1\n\t" \
  "s_or_b32 s80, s62, s68\n\t" \
  "s_cselect_b32 s80, 0, 200\n\t" \
  "s_cmp_lt_u32 s21, 3\n\t" \
  "s_cselect_b32 s80, 0, s80\n\t" \
  "s_add_i32 s17, s17, s80\n\t" \
  "s_cmp_eq_u32 s62, 0\n\t" \
  "s_cbranch_scc1 CH_NOUPD\n\t" \
  "s_max_i32 s62, s70, s69\n\t" \
  "s_sub_i32 s34, s34, s62\n\t" \
  "s_sub_i32 s34, s34, s17\n\t" \
  "s_add_i32 s62, s34, 16\n\t" \
  "s_ashr_i32 s62, s62, 2\n\t" \
  "s_min_i32 s62, s62, 0\n\t" \
  "s_min_i32 s34, s34, 200\n\t" \
  "s_cmp_gt_i32 s34, 0\n\t" \
  "s_cselect_b32 s34, s34, s62\n\t" \
  "s_add_i32 s17, s17, s34\n\t" \
  "s_sub_i32 s70, s70, s69\n\t" \
  "s_add_i32 s62, s70, 16\n\t" \
  "s_ashr_i32 s62, s62, 2\n\t" \
  "s_min_i32 s62, s62, 0\n\t" \
  "s_min_i32 s70, s70, 200\n\t" \
  "s_cmp_gt_i32 s70, 0\n\t" \
  "s_cselect_b32 s70, s70, s62\n\t" \
  "s_add_i32 s69, s69, s70\n\t" \
  "CH_NOUPD:\n\t" \
  "s_add_i32 s58, s61, s17\n\t" \
  "s_add_i32 s58, s58, s39\n\t" \
  "s_add_i32 s58, s58, s69\n\t" \
  "s_add_i32 s58, s58, s59\n\t" \
  "CH_NOCTL:\n\t" \
  "s_waitcnt lgkmcnt(0)\n\t" \
  "s_add_u32 s28, s36, s65\n\t" \
  "s_add_u32 s34, s28, 1700\n\t" \
  "s_cmp_eq_u32 s21, 0\n\t" \
  "s_cselect_b32 s58, s34, s58\n\t" \
  "s_add_u32 s29, s28, s59\n\t" \
  "s_add_u32 s34, s61, s69\n\t" \
  "s_sub_u32 s34, s34, s29\n\t" \
  "s_max_i32 s34, s34, 0\n\t" \
  "s_cmp_eq_u32 s21, 0\n\t" \
  "s_cselect_b32 s34, 0, s34\n\t" \
  "s_add_u32 s29, s29, s34\n\t" \
  "s_add_u32 s29, s29, s16\n\t" \
  "s_barrier\n\t" \
  "s_bitcmp1_b32 s18, 0\n\t" \
  "s_cbranch_scc0 CH_NORED\n\t" \
  "s_mov_b32 s30, 0\n\t" \
  "v_mov_b32 v175, s28\n\t" \
  "s_sub_u32 s29, s29, s65\n\t" \
  "s_sub_u32 s29, s29, 40\n\t" \
  "CH_W1:\n\t" \
  "s_memtime s[36:37]\n\t" \
  "s_waitcnt lgkmcnt(0)\n\t" \
  "s_sub_u32 s62, s29, s36\n\t" \
  "s_sub_u32 s34, s62, 121\n\t" \
  "s_cmp_lt_u32 s34, 0x8000\n\t" \
  "s_cbranch_scc1 CH_W1\n\t" \
  "s_cmp_lt_i32 s62, 64\n\t" \
  "s_cbranch_scc1 CH_W164\n\t" \
  "s_sub_u32 s62, s62, 64\n\t" \
  "s_nop 13\n\t" \
  "CH_W164:\n\t" \
  "s_cmp_lt_i32 s62, 32\n\t" \
  "s_cbranch_scc1 CH_W132\n\t" \
  "s_sub_u32 s62, s62, 32\n\t" \
  "s_nop 5\n\t" \
  "CH_W132:\n\t" \
  "s_cmp_lt_i32 s62, 16\n\t" \
  "s_cbranch_scc1 CH_W116\n\t" \
  "s_sub_u32 s62, s62, 16\n\t" \
  "s_nop 1\n\t" \
  "CH_W116:\n\t" \
  "buffer_load_dwordx4 v[96:99], v14, s[8:11], s26 offen sc1\n\t" \
  "buffer_load_dwordx4 v[100:103], v14, s[8:11], s26 offen offset:1024 sc1\n\t" \
  "buffer_load_dwordx4 v[104:107], v14, s[8:11], s26 offen offset:2048 sc1\n\t" \
  "buffer_load_dwordx4 v[108:111], v14, s[8:11], s26 offen offset:3072 sc1\n\t" \
  "ds_read_b128 v[32:35], v1 offset:0\n\t" \
  "ds_read_b128 v[36:39], v1 offset:1024\n\t" \
  "ds_read_b128 v[40:43], v1 offset:2048\n\t" \
  "ds_read_b128 v[44:47], v1 offset:3072\n\t" \
  "ds_read_b128 v[48:51], v1 offset:4096\n\t" \
  "ds_read_b128 v[52:55], v1 offset:5120\n\t" \
  "ds_read_b128 v[56:59], v1 offset:6144\n\t" \
  "ds_read_b128 v[60:63], v1 offset:7168\n\t" \
  "s_add_u32 s34, s21, 1\n\t" \
  "s_lshl_b32 s22, s34, 15\n\t" \
  "s_add_u32 s22, s22, s79\n\t" \
  "s_add_u32 s34, s34, 1\n\t" \
  "s_min_u32 s34, s34, 63\n\t" \
  "s_lshl_b32 s34, s34, 18\n\t" \
  "s_add_u32 s23, s12, s34\n\t" \
  "s_lshl_b32 s62, s35, 2\n\t" \
  "s_add_u32 s23, s23, s62\n\t" \
  "s_add_u32 s25, s13, s34\n\t" \
  "s_mov_b32 s27, s34\n\t" \
  "s_sub_u32 s34, s19, s34\n\t" \
  "v_writelane_b32 v13, s34, 32\n\t" \
  "s_mov_b32 s84, s71\n\t" \
  "s_mov_b32 s71, s26\n\t" \
  "s_add_u32 s26, s26, 0xa0000\n\t" \
  "s_cmp_eq_u32 s26, s78\n\t" \
  "s_cselect_b32 s26, s77, s26\n\t" \
  "s_add_u32 s54, s26, 0x4000\n\t" \
  "s_add_u32 s55, s26, 0x8000\n\t" \
  "ds_read_b128 v[132:135], v2 offset:0\n\t" \
  "ds_read_b128 v[136:139], v2 offset:1024\n\t" \
  "ds_read_b128 v[140:143], v2 offset:2048\n\t" \
  "ds_read_b128 v[144:147], v2 offset:3072\n\t" \
  "ds_read_b128 v[148:151], v2 offset:4096\n\t" \
  "ds_read_b128 v[152:155], v2 offset:5120\n\t" \
  "ds_read_b128 v[156:159], v2 offset:6144\n\t" \
  "s_waitcnt lgkmcnt(14)\n\t" \
  "ds_read_b128 v[160:163], v2 offset:7168\n\t" \
  "s_waitcnt lgkmcnt(14)\n\t" \
  "ds_read_b128 v[164:167], v2 offset:8192\n\t" \
  "s_waitcnt lgkmcnt(14)\n\t" \
  "ds_read_b128 v[168:171], v2 offset:9216\n\t" \
  "v_mov_b64 v[72:73], 0\n\t" \
  "v_mov_b64 v[74:75], 0\n\t" \
  "v_mov_b64 v[76:77], 0\n\t" \
  "v_mov_b64 v[78:79], 0\n\t" \
  "v_mov_b64 v[80:81], 0\n\t" \
  "v_mov_b64 v[82:83], 0\n\t" \
  "v_mov_b64 v[84:85], 0\n\t" \
  "v_mov_b64 v[86:87], 0\n\t" \
  "v_mov_b64 v[88:89], 0\n\t" \
  "v_mov_b64 v[90:91], 0\n\t" \
  "v_mov_b64 v[92:93], 0\n\t" \
  "v_mov_b64 v[94:95], 0\n\t" \
  "s_waitcnt lgkmcnt(0)\n\t" \
  "s_bitcmp1_b32 s18, 1\n\t" \
  "s_cbranch_scc0 CH_R1B\n\t" \
  "s_bitcmp1_b32 s18, 1\n\t" \
  "s_cbranch_scc0 CH_NOPUBA\n\t" \
  "v_add_u32 v30, s35, v4\n\t" \
  "ds_read_b64_tr_b16 v[124:125], v30 offset:13312\n\t" \
  "ds_read_b64_tr_b16 v[126:127], v30 offset:13568\n\t" \
  "s_lshl_b32 s34, s21, 16\n\t" \
  "s_add_u32 s29, s34, s15\n\t" \
  "buffer_store_dwordx2 v[116:117], v21, s[8:11], s29 offen sc1\n\t" \
  "s_add_u32 s29, s34, s14\n\t" \
  "s_waitcnt lgkmcnt(0)\n\t" \
  "buffer_store_dwordx4 v[124:127], v20, s[8:11], s29 offen sc1\n\t" \
  "s_nop 1\n\t" \
  "CH_NOPUBA:\n\t" \
  "s_waitcnt vmcnt(5)\n\t" \
  "v_mfma_f32_16x16x32_bf16 v[112:115], v[16:19], v[96:99], 0\n\t" \
  "v_max3_u32 v173, v96, v97, v98\n\t" \
  "v_mov_b32 v174, v99\n\t" \
  "s_waitcnt vmcnt(4)\n\t" \
  "v_mfma_f32_16x16x32_bf16 v[112:115], v[16:19], v[100:103], v[112:115]\n\t" \
  "v_max3_u32 v173, v173, v100, v101\n\t" \
  "v_max3_u32 v174, v174, v102, v103\n\t" \
  "s_waitcnt vmcnt(3)\n\t" \
  "v_mfma_f32_16x16x32_bf16 v[112:115], v[16:19], v[104:107], v[112:115]\n\t" \
  "v_max3_u32 v173, v173, v104, v105\n\t" \
  "v_max3_u32 v174, v174, v106, v107\n\t" \
  "s_waitcnt vmcnt(2)\n\t" \
  "v_mfma_f32_16x16x32_bf16 v[112:115], v[16:19], v[108:111], v[112:115]\n\t" \
  "s_memtime s[36:37]\n\t" \
  "v_max3_u32 v173, v173, v108, v109\n\t" \
  "v_max3_u32 v174, v174, v110, v111\n\t" \
  "v_max_u32 v173, v173, v174\n\t" \
  "v_cmp_eq_u32 vcc, -1, v173\n\t" \
  "s_cbranch_vccnz CH_F1\n\t" \
  "s_nop 1\n\t" \
  "s_branch CH_OK1\n\t" \
  "CH_R1:\n\t" \
  "buffer_load_dwordx4 v[96:99], v14, s[8:11], s71 offen offset:0 sc1\n\t" \
  "buffer_load_dwordx4 v[100:103], v14, s[8:11], s71 offen offset:1024 sc1\n\t" \
  "buffer_load_dwordx4 v[104:107], v14, s[8:11], s71 offen offset:2048 sc1\n\t" \
  "buffer_load_dwordx4 v[108:111], v14, s[8:11], s71 offen offset:3072 sc1\n\t" \
  "CH_R1B:\n\t" \
  "s_waitcnt vmcnt(3)\n\t" \
  "v_mfma_f32_16x16x32_bf16 v[112:115], v[16:19], v[96:99], 0\n\t" \
  "v_max3_u32 v173, v96, v97, v98\n\t" \
  "v_mov_b32 v174, v99\n\t" \
  "s_waitcnt vmcnt(2)\n\t" \
  "v_mfma_f32_16x16x32_bf16 v[112:115], v[16:19], v[100:103], v[112:115]\n\t" \
  "v_max3_u32 v173, v173, v100, v101\n\t" \
  "v_max3_u32 v174, v174, v102, v103\n\t" \
  "s_waitcnt vmcnt(1)\n\t" \
  "v_mfma_f32_16x16x32_bf16 v[112:115], v[16:19], v[104:107], v[112:115]\n\t" \
  "v_max3_u32 v173, v173, v104, v105\n\t" \
  "v_max3_u32 v174, v174, v106, v107\n\t" \
  "s_waitcnt vmcnt(0)\n\t" \
  "v_mfma_f32_16x16x32_bf16 v[112:115], v[16:19], v[108:111], v[112:115]\n\t" \
  "s_memtime s[36:37]\n\t" \
  "v_max3_u32 v173, v173, v108, v109\n\t" \
  "v_max3_u32 v174, v174, v110, v111\n\t" \
  "v_max_u32 v173, v173, v174\n\t" \
  "v_cmp_eq_u32 vcc, -1, v173\n\t" \
  "s_cbranch_vccnz CH_F1\n\t" \
  "CH_OK1:\n\t" \
  "s_nop 1\n\t" \
  "v_add_f32_dpp v30, v112, v112 row_ror:8 row_mask:0xf bank_mask:0xf\n\t" \
  "v_add_f32_dpp v31, v113, v113 row_ror:8 row_mask:0xf bank_mask:0xf\n\t" \
  "v_cvt_pk_bf16_f32 v30, v30, v31\n\t" \
  "v_min_u32 v30, -2, v30\n\t" \
  "buffer_store_dword v30, v15, s[8:11], s22 offen sc1\n\t" \
  "s_waitcnt lgkmcnt(0)\n\t" \
  "s_add_u32 s64, s36, s65\n\t" \
  "v_mov_b32 v174, s64\n\t" \
  "buffer_store_dwordx2 v[174:175], v22, s[8:11], s22 offen sc1\n\t" \
  "s_branch CH_RED_DONE\n\t" \
  "CH_NORED:\n\t" \
  "v_mov_b64 v[96:97], -1\n\t" \
  "v_mov_b64 v[98:99], -1\n\t" \
  "ds_read_b128 v[32:35], v1 offset:0\n\t" \
  "ds_read_b128 v[36:39], v1 offset:1024\n\t" \
  "ds_read_b128 v[40:43], v1 offset:2048\n\t" \
  "ds_read_b128 v[44:47], v1 offset:3072\n\t" \
  "ds_read_b128 v[48:51], v1 offset:4096\n\t" \
  "ds_read_b128 v[52:55], v1 offset:5120\n\t" \
  "ds_read_b128 v[56:59], v1 offset:6144\n\t" \
  "ds_read_b128 v[60:63], v1 offset:7168\n\t" \
  "s_add_u32 s34, s21, 1\n\t" \
  "s_lshl_b32 s22, s34, 15\n\t" \
  "s_add_u32 s22, s22, s79\n\t" \
  "s_add_u32 s34, s34, 1\n\t" \
  "s_min_u32 s34, s34, 63\n\t" \
  "s_lshl_b32 s34, s34, 18\n\t" \
  "s_add_u32 s23, s12, s34\n\t" \
  "s_lshl_b32 s62, s35, 2\n\t" \
  "s_add_u32 s23, s23, s62\n\t" \
  "s_add_u32 s25, s13, s34\n\t" \
  "s_mov_b32 s27, s34\n\t" \
  "s_sub_u32 s34, s19, s34\n\t" \
  "v_writelane_b32 v13, s34, 32\n\t" \
  "s_mov_b32 s84, s71\n\t" \
  "s_mov_b32 s71, s26\n\t" \
  "s_add_u32 s26, s26, 0xa0000\n\t" \
  "s_cmp_eq_u32 s26, s78\n\t" \
  "s_cselect_b32 s26, s77, s26\n\t" \
  "s_add_u32 s54, s26, 0x4000\n\t" \
  "s_add_u32 s55, s26, 0x8000\n\t" \
  "ds_read_b128 v[132:135], v2 offset:0\n\t" \
  "ds_read_b128 v[136:139], v2 offset:1024\n\t" \
  "ds_read_b128 v[140:143], v2 offset:2048\n\t" \
  "ds_read_b128 v[144:147], v2 offset:3072\n\t" \
  "ds_read_b128 v[148:151], v2 offset:4096\n\t" \
  "ds_read_b128 v[152:155], v2 offset:5120\n\t" \
  "ds_read_b128 v[156:159], v2 offset:6144\n\t" \
  "s_waitcnt lgkmcnt(14)\n\t" \
  "ds_read_b128 v[160:163], v2 offset:7168\n\t" \
  "s_waitcnt lgkmcnt(14)\n\t" \
  "ds_read_b128 v[164:167], v2 offset:8192\n\t" \
  "s_waitcnt lgkmcnt(14)\n\t" \
  "ds_read_b128 v[168:171], v2 offset:9216\n\t" \
  "v_mov_b64 v[72:73], 0\n\t" \
  "v_mov_b64 v[74:75], 0\n\t" \
  "v_mov_b64 v[76:77], 0\n\t" \
  "v_mov_b64 v[78:79], 0\n\t" \
  "v_mov_b64 v[80:81], 0\n\t" \
  "v_mov_b64 v[82:83], 0\n\t" \
  "v_mov_b64 v[84:85], 0\n\t" \
  "v_mov_b64 v[86:87], 0\n\t" \
  "v_mov_b64 v[88:89], 0\n\t" \
  "v_mov_b64 v[90:91], 0\n\t" \
  "v_mov_b64 v[92:93], 0\n\t" \
  "v_mov_b64 v[94:95], 0\n\t" \
  "s_waitcnt lgkmcnt(0)\n\t" \
  "s_bitcmp1_b32 s18, 2\n\t" \
  "s_cbranch_scc0 CH_NOCAL\n\t" \
  "s_mov_b32 s82, s65\n\t" \
  "s_mov_b32 s80, s76\n\t" \
  "s_mov_b32 s65, s76\n\t" \
  "s_mov_b32 s62, 2\n\t" \
  "CH_CAL:\n\t" \
  "s_memrealtime s[66:67]\n\t" \
  "s_memtime s[36:37]\n\t" \
  "s_waitcnt lgkmcnt(0)\n\t" \
  "s_mul_i32 s34, s66, 24\n\t" \
  "s_sub_u32 s34, s34, s36\n\t" \
  "s_sub_u32 s34, s34, s65\n\t" \
  "s_max_i32 s34, s34, 0\n\t" \
  "s_add_u32 s65, s65, s34\n\t" \
  "s_sub_u32 s62, s62, 1\n\t" \
  "s_cmp_gt_i32 s62, 0\n\t" \
  "s_cbranch_scc1 CH_CAL\n\t" \
  "s_sub_i32 s80, s65, s80\n\t" \
  "s_sub_i32 s80, s80, s81\n\t" \
  "s_ashr_i32 s80, s80, 2\n\t" \
  "s_cmp_lt_u32 s21, 2\n\t" \
  "s_cselect_b32 s80, 0, s80\n\t" \
  "s_add_i32 s80, s80, s81\n\t" \
  "v_mov_b32 v30, s65\n\t" \
  "v_mov_b32 v31, s80\n\t" \
  "v_mov_b32 v174, 0\n\t" \
  "ds_write_b64 v174, v[30:31] offset:9216\n\t" \
  "s_mov_b32 s65, s82\n\t" \
  "CH_NOCAL:\n\t" \
  "s_add_u32 s29, s29, 80\n\t" \
  "s_sub_u32 s29, s29, s65\n\t" \
  "s_sub_u32 s29, s29, 40\n\t" \
  "CH_W3:\n\t" \
  "s_memtime s[36:37]\n\t" \
  "s_waitcnt lgkmcnt(0)\n\t" \
  "s_sub_u32 s62, s29, s36\n\t" \
  "s_sub_u32 s34, s62, 121\n\t" \
  "s_cmp_lt_u32 s34, 0x8000\n\t" \
  "s_cbranch_scc1 CH_W3\n\t" \
  "s_cmp_lt_i32 s62, 64\n\t" \
  "s_cbranch_scc1 CH_W364\n\t" \
  "s_sub_u32 s62, s62, 64\n\t" \
  "s_nop 13\n\t" \
  "CH_W364:\n\t" \
  "s_cmp_lt_i32 s62, 32\n\t" \
  "s_cbranch_scc1 CH_W332\n\t" \
  "s_sub_u32 s62, s62, 32\n\t" \
  "s_nop 5\n\t" \
  "CH_W332:\n\t" \
  "s_cmp_lt_i32 s62, 16\n\t" \
  "s_cbranch_scc1 CH_W316\n\t" \
  "s_sub_u32 s62, s62, 16\n\t" \
  "s_nop 1\n\t" \
  "CH_W316:\n\t" \
  "s_bitcmp1_b32 s18, 1\n\t" \
  "s_cbranch_scc0 CH_NOPUBC\n\t" \
  "v_add_u32 v30, s35, v4\n\t" \
  "ds_read_b64_tr_b16 v[100:101], v30 offset:13312\n\t" \
  "ds_read_b64_tr_b16 v[102:103], v30 offset:13568\n\t" \
  "s_lshl_b32 s34, s21, 16\n\t" \
  "s_add_u32 s29, s34, s15\n\t" \
  "buffer_store_dwordx2 v[116:117], v21, s[8:11], s29 offen sc1\n\t" \
  "s_add_u32 s29, s34, s14\n\t" \
  "s_waitcnt lgkmcnt(0)\n\t" \
  "buffer_store_dwordx4 v[100:103], v20, s[8:11], s29 offen sc1\n\t" \
  "s_nop 1\n\t" \
  "CH_NOPUBC:\n\t" \
  "buffer_store_dwordx4 v[96:99], v14, s[8:11], s84 offen sc1\n\t" \
  "buffer_store_dwordx4 v[96:99], v14, s[8:11], s84 offen offset:1024 sc1\n\t" \
  "buffer_store_dwordx4 v[96:99], v14, s[8:11], s84 offen offset:2048 sc1\n\t" \
  "buffer_store_dwordx4 v[96:99], v14, s[8:11], s84 offen offset:3072 sc1\n\t" \
  "s_nop 1\n\t" \
  "s_branch CH_NEXT\n\t" \
  "CH_RED_DONE:\n\t" \
  "s_nop 1\n\t" \
  "CH_NEXT:\n\t" \
  "s_add_u32 s21, s21, 1\n\t" \
  "s_branch CH_TOP\n\t" \
  "CH_F2:\n\t" \
  "s_add_u32 s32, s32, 1\n\t" \
  "s_add_u32 s30, s30, 1\n\t" \
  "s_cmp_lg_u32 s33, 0\n\t" \
  "s_cbranch_scc1 CH_A\n\t" \
  "s_waitcnt lgkmcnt(0)\n\t" \
  "s_cmp_lt_u32 s30, 0x10000\n\t" \
  "s_cbranch_scc1 CH_R2\n\t" \
  "s_mov_b32 s33, 1\n\t" \
  "s_branch CH_A\n\t" \
  "CH_FK:\n\t" \
  "s_cmp_eq_u32 s21, 63\n\t" \
  "s_cbranch_scc1 CH_KOK\n\t" \
  "s_mov_b32 s30, 0\n\t" \
  "CH_FK2:\n\t" \
  "s_cmp_lg_u32 s33, 0\n\t" \
  "s_cbranch_scc1 CH_KOK\n\t" \
  "buffer_load_dword v172, v13, s[8:11], s27 offen sc1\n\t" \
  "s_waitcnt vmcnt(0)\n\t" \
  "v_readlane_b32 s34, v172, 32\n\t" \
  "s_add_u32 s30, s30, 1\n\t" \
  "s_add_u32 s31, s31, 0x10000\n\t" \
  "s_cmp_gt_i32 s34, s21\n\t" \
  "s_cbranch_scc1 CH_KOK\n\t" \
  "s_cmp_lt_u32 s30, 0x10000\n\t" \
  "s_cbranch_scc1 CH_FK2\n\t" \
  "s_mov_b32 s33, 1\n\t" \
  "s_branch CH_KOK\n\t" \
  "CH_F1:\n\t" \
  "s_add_u32 s31, s31, 1\n\t" \
  "s_add_u32 s30, s30, 1\n\t" \
  "s_nop 7\n\t" \
  "s_waitcnt lgkmcnt(0)\n\t" \
  "s_cmp_lg_u32 s33, 0\n\t" \
  "s_cbranch_scc1 CH_OK1\n\t" \
  "s_cmp_lt_u32 s30, 0x10000\n\t" \
  "s_cbranch_scc1 CH_R1\n\t" \
  "s_mov_b32 s33, 1\n\t" \
  "s_branch CH_OK1\n\t" \
  "CH_LAST:\n\t" \
  "s_nop 7\n\t" \
  "s_bitcmp1_b32 s18, 1\n\t" \
  "s_cbranch_scc0 CH_NOPUBB\n\t" \
  "v_add_u32 v30, s35, v4\n\t" \
  "ds_read_b64_tr_b16 v[100:101], v30 offset:13312\n\t" \
  "ds_read_b64_tr_b16 v[102:103], v30 offset:13568\n\t" \
  "s_lshl_b32 s34, s21, 16\n\t" \
  "s_add_u32 s29, s34, s15\n\t" \
  "buffer_store_dwordx2 v[116:117], v21, s[8:11], s29 offen sc1\n\t" \
  "s_add_u32 s29, s34, s14\n\t" \
  "s_waitcnt lgkmcnt(0)\n\t" \
  "buffer_store_dwordx4 v[100:103], v20, s[8:11], s29 offen sc1\n\t" \
  "s_nop 1\n\t" \
  "CH_NOPUBB:\n\t" \
  "s_waitcnt vmcnt(0) lgkmcnt(0)\n\t"
#define CHAIN_CLOBBERS "v24", "v25", "v26", "v27", "v28", "v29", "v30", "v31", "v32", "v33", "v34", "v35", "v36", "v37", "v38", "v39", "v40", "v41", "v42", "v43", "v44", "v45", "v46", "v47", "v48", "v49", "v50", "v51", "v52", "v53", "v54", "v55", "v56", "v57", "v58", "v59", "v60", "v61", "v62", "v63", "v64", "v65", "v66", "v67", "v68", "v69", "v70", "v71", "v72", "v73", "v74", "v75", "v76", "v77", "v78", "v79", "v80", "v81", "v82", "v83", "v84", "v85", "v86", "v87", "v88", "v89", "v90", "v91", "v92", "v93", "v94", "v95", "v96", "v97", "v98", "v99", "v100", "v101", "v102", "v103", "v104", "v105", "v106", "v107", "v108", "v109", "v110", "v111", "v112", "v113", "v114", "v115", "v116", "v117", "v118", "v119", "v120", "v121", "v122", "v123", "v124", "v125", "v126", "v127", "v128", "v129", "v130", "v131", "v132", "v133", "v134", "v135", "v136", "v137", "v138", "v139", "v140", "v141", "v142", "v143", "v144", "v145", "v146", "v147", "v148", "v149", "v150", "v151", "v152", "v153", "v154", "v155", "v156", "v157", "v158", "v159", "v160", "v161", "v162", "v163", "v164", "v165", "v166", "v167", "v168", "v169", "v170", "v171", "v172", "v173", "v174", "v175", "s13", "s14", "s15", "s19", "s20", "s21", "s22", "s23", "s25", "s26", "s27", "s28", "s29", "s30", "s34", "s36", "s37", "s38", "s52", "s53", "s54", "s55", "s56", "s57", "s58", "s59", "s60", "s61", "s62", "s63", "s64", "s65", "s66", "s67", "s68", "s69", "s70", "s71", "s72", "s73", "s75", "s76", "s78", "s79", "s80", "s81", "s82", "s83", "s84", "s85", "vcc", "scc", "memory"

constexpr int DM = 576, DH = 128, NSEG = 64, HD = 4096;
constexpr u32 SENT = 0xFFFFFFFFu;
constexpr int OOB = (int)0x80000000u;
constexpr int SC1 = 16;

constexpr int MB = 1 << 20;
constexpr int OFF_KX = 0;
constexpr int OFF_VX = 16 * MB;
constexpr int OFF_KM = 32 * MB;
constexpr int OFF_VM = 36 * MB;
constexpr int OFF_O = 40 * MB;
constexpr int OFF_SLAB = 56 * MB;
constexpr int OFF_KVP = 76 * MB;
constexpr int OFF_OB = 78 * MB;
constexpr int OFF_BT = 82 * MB;
constexpr int OFF_G = 88 * MB;
constexpr int OFF_DBG = 89 * MB;
constexpr int OFF_FL = 92 * MB;
constexpr int OFF_HO = 96 * MB;
constexpr int FL_KV = OFF_FL, FL_O = OFF_FL + 8192, FL_PAR = OFF_FL + 16384, FL_B1 = FL_PAR + 256, FL_B2 = FL_PAR + 512, FL_B3 = FL_PAR + 768,
              FL_ERR = FL_PAR + 1024, FL_ARM = FL_PAR + 1280, FL_BT = FL_PAR + 2048, FL_TK = OFF_FL + 20480;
constexpr int FL_BYTES = 20480 + 64 * 12 * 64;
constexpr int SPIN_CAP = 1 << 18;

__device__ __forceinline__ float bf2f(u16 b) { return __uint_as_float((u32)b << 16); }
__device__ __forceinline__ float bflo(u32 b) { return __uint_as_float(b << 16); }
__device__ __forceinline__ float bfhi(u32 b) { return __uint_as_float(b & 0xffff0000u); }
__device__ __forceinline__ u32 pk2(float a, float b) { f32x2 f = {a, b}; return __builtin_bit_cast(u32, __builtin_convertvector(f, bf16x2)); }
__device__ __forceinline__ float rbf(float a) { return bflo(pk2(a, 0.f)); }
__device__ __forceinline__ f32x4 mfma32(v4u a, v4u b, f32x4 c) {
  return __builtin_amdgcn_mfma_f32_16x16x32_bf16(__builtin_bit_cast(bf16x8, a), __builtin_bit_cast(bf16x8, b), c, 0, 0, 0);
}
__device__ __forceinline__ f32x4 mfma16(v2u a, v2u b, f32x4 c) {
  return __builtin_amdgcn_mfma_f32_16x16x16bf16_1k(__builtin_bit_cast(s16x4, a), __builtin_bit_cast(s16x4, b), c, 0, 0, 0);
}
__device__ __forceinline__ v2u trread(const LAS unsigned char* p) {
  return __builtin_bit_cast(v2u, __builtin_amdgcn_ds_read_tr16_b64_v4i16((LAS s16x4*)p));
}
__device__ __forceinline__ int amap(int off, int par) { return ((off >> 13) << 14) | (par << 13) | (off & 8191); }
__device__ __forceinline__ float xmax(float a) {
  auto r = __builtin_amdgcn_permlane16_swap(__float_as_uint(a), __float_as_uint(a), false, false); a = fmaxf(__uint_as_float(r[0]), __uint_as_float(r[1]));
  auto q = __builtin_amdgcn_permlane32_swap(__float_as_uint(a), __float_as_uint(a), false, false); return fmaxf(__uint_as_float(q[0]), __uint_as_float(q[1]));
}
__device__ __forceinline__ float xsum(float a) {
  auto r = __builtin_amdgcn_permlane16_swap(__float_as_uint(a), __float_as_uint(a), false, false); a = __uint_as_float(r[0]) + __uint_as_float(r[1]);
  auto q = __builtin_amdgcn_permlane32_swap(__float_as_uint(a), __float_as_uint(a), false, false); return __uint_as_float(q[0]) + __uint_as_float(q[1]);
}
__device__ __forceinline__ int lane_id() { int l; asm volatile("v_mbcnt_lo_u32_b32 %0, -1, 0\n\tv_mbcnt_hi_u32_b32 %0, -1, %0" : "=v"(l)); return l; }
#define SB() __builtin_amdgcn_sched_barrier(0)
#define SGB(mask, cnt) __builtin_amdgcn_sched_group_barrier(mask, cnt, 0)
#define DRAIN() asm volatile("s_waitcnt vmcnt(0)" ::: "memory")
#define RLX_AGENT __ATOMIC_RELAXED, __HIP_MEMORY_SCOPE_AGENT
typedef __amdgpu_buffer_rsrc_t rsrc_t;
#define MKRS(p, bytes) __builtin_amdgcn_make_buffer_rsrc((void*)(p), 0, (bytes), 0x00020000)
__device__ __forceinline__ v4u ld128(rsrc_t r, int vo, int so) { return __builtin_amdgcn_raw_buffer_load_b128(r, vo, so, SC1); }
__device__ __forceinline__ void st128(v4u v, rsrc_t r, int vo, int so) { __builtin_amdgcn_raw_buffer_store_b128(v, r, (int)((u32)vo + (u32)so), 0, SC1); }

#define TS(i) do { } while (0)
struct Args {
  const u16* x; const u16* mem0; const u16* Wq; const u16* Wk; const u16* Wv; const u16* Wo; const u16* wqkv; const u16* wo; const u16* gw;
  u16* out; unsigned char* ws;
  int d1, r0, a0, delta;
};

__device__ __forceinline__ int ho_off(const unsigned char* ws) { return OFF_HO + (int)((0u - (u32)(uintptr_t)ws) & (2u * MB - 1u)); }
__device__ __forceinline__ void wait_ge(unsigned char* ws, int off, int want) {
  GAS int* p = (GAS int*)(ws + off);
  for (int sp = 0;; ++sp) {
    if (__builtin_amdgcn_readfirstlane(__hip_atomic_load(p, RLX_AGENT)) >= want) break;
    if (sp > SPIN_CAP) { __hip_atomic_store((GAS int*)(ws + FL_ERR), off, RLX_AGENT); break; }
    if ((sp & 255) == 255 && __builtin_amdgcn_readfirstlane(__hip_atomic_load((GAS int*)(ws + FL_ERR), RLX_AGENT)) != -1) break;
    __builtin_amdgcn_s_sleep(2);
  }
  __builtin_amdgcn_fence(__ATOMIC_ACQUIRE, "wavefront");
}
__device__ __forceinline__ void publish_set(unsigned char* ws, int off, int val, bool lead) {
  DRAIN(); __syncthreads();
  if (lead) __hip_atomic_store((GAS int*)(ws + off), val, RLX_AGENT);
}
__device__ __forceinline__ void publish_add(unsigned char* ws, int off, bool lead) {
  DRAIN(); __syncthreads();
  if (lead) __hip_atomic_fetch_add((GAS int*)(ws + off), 1, RLX_AGENT);
}

constexpr int CH_LDS_MS = 0;
constexpr int CH_LDS_OF = CH_LDS_MS + 9216;
constexpr int CH_LDS_IMG = CH_LDS_OF + 2048;
constexpr int CH_LDS_ST = CH_LDS_IMG + 4096;
constexpr int CH_LDS_WV = CH_LDS_ST + 1024;
constexpr int LDS_BYTES = CH_LDS_WV + 147456;

__device__ __forceinline__ u32 probe_once(unsigned char* p) {
  unsigned long long c0, c1;
  asm volatile("s_memtime %0\n\ts_waitcnt lgkmcnt(0)\n\tglobal_store_dword %2, %3, off sc1\n\ts_waitcnt vmcnt(0)\n\ts_memtime %1\n\ts_waitcnt lgkmcnt(0)"
               : "=&s"(c0), "=&s"(c1) : "v"(p), "v"(SENT) : "memory");
  return (u32)(c1 - c0);
}

__device__ __forceinline__ void chain_role(const Args& a, LAS unsigned char* lds, int w, int idx) {
  const int r3 = (idx >> 2) / 3, h = 8 * (idx & 3) + r3, cs = (idx >> 2) - 3 * r3;
  const int lane = lane_id();
  const int n = lane & 15, g = lane >> 4, th = lane >> 5;
  const rsrc_t rs = MKRS(a.ws, 0x7fffffff);
  const int OFF_P0 = ho_off(a.ws);
  v32u wqk[9];
  const int drow = 16 * (g >> 1) + 4 * (g & 1);
  {
    const rsrc_t rw = MKRS(w < 2 ? a.Wq : a.Wk, DM * HD * 2);
    int vo = (drow * HD + h * DH + 64 * (w & 1) + 4 * n) * 2;
    v2u lx[5][16];
#define ISSUEQK(slot) do { \
      _Pragma("unroll") for (int p = 0; p < 8; ++p) { \
        const int d0 = 32 * (p >> 2) + 8 * (p & 1) + 2 * ((p >> 1) & 1); \
        lx[slot][2 * p] = __builtin_amdgcn_raw_buffer_load_b64(rw, vo, d0 * HD * 2, 0); lx[slot][2 * p + 1] = __builtin_amdgcn_raw_buffer_load_b64(rw, vo, (d0 + 1) * HD * 2, 0); } \
      vo += 64 * HD * 2; SB(); } while (0)
#define PACKQK(slot, ks, PIN) do { v32u t_; \
      _Pragma("unroll") for (int p = 0; p < 8; ++p) { const v2u x = lx[slot][2 * p], y = lx[slot][2 * p + 1]; \
        t_[p] = __builtin_amdgcn_perm(y.x, x.x, 0x05040100u); t_[8 + p] = __builtin_amdgcn_perm(y.x, x.x, 0x07060302u); \
        t_[16 + p] = __builtin_amdgcn_perm(y.y, x.y, 0x05040100u); t_[24 + p] = __builtin_amdgcn_perm(y.y, x.y, 0x07060302u); } \
      asm volatile("" : PIN(wqk[ks]) : "0"(t_)); SB(); } while (0)
    ISSUEQK(0); ISSUEQK(1); ISSUEQK(2);
    PACKQK(0, 0, "={a[0:31]}"); ISSUEQK(3); PACKQK(1, 1, "={a[32:63]}"); ISSUEQK(4); PACKQK(2, 2, "={a[64:95]}"); ISSUEQK(0);
    PACKQK(3, 3, "={a[96:127]}"); ISSUEQK(1); PACKQK(4, 4, "={a[128:159]}"); ISSUEQK(2); PACKQK(0, 5, "={a[160:191]}"); ISSUEQK(3);
    PACKQK(1, 6, "={a[192:223]}"); PACKQK(2, 7, "={a[224:255]}"); PACKQK(3, 8, "={v[176:207]}");
  }
  TS(0);
  v32u WVA; v16u WVB;
  {
    const rsrc_t rw = MKRS(a.Wv, DM * HD * 2);
    int vo = (drow * HD + h * DH + 32 * w + 2 * n) * 2;
    u32 lv[9][16];
#pragma unroll
    for (int ks = 0; ks < 9; ++ks) {
#pragma unroll
      for (int p = 0; p < 8; ++p) {
        const int d0 = 32 * (p >> 2) + 8 * (p & 1) + 2 * ((p >> 1) & 1);
        lv[ks][2 * p] = __builtin_amdgcn_raw_buffer_load_b32(rw, vo, d0 * HD * 2, 0); lv[ks][2 * p + 1] = __builtin_amdgcn_raw_buffer_load_b32(rw, vo, (d0 + 1) * HD * 2, 0);
      }
      vo += 64 * HD * 2;
    }
    SB();
#pragma unroll
    for (int ks = 0; ks < 9; ++ks) {
      v8u f0, f1;
#pragma unroll
      for (int p = 0; p < 8; ++p) {
        f0[p] = __builtin_amdgcn_perm(lv[ks][2 * p + 1], lv[ks][2 * p], 0x05040100u);
        f1[p] = __builtin_amdgcn_perm(lv[ks][2 * p + 1], lv[ks][2 * p], 0x07060302u);
      }
      if (ks < 2) {
#pragma unroll
        for (int p = 0; p < 8; ++p) { WVA[16 * ks + p] = f0[p]; WVA[16 * ks + 8 + p] = f1[p]; }
      } else if (ks == 2) {
#pragma unroll
        for (int p = 0; p < 8; ++p) { WVB[p] = f0[p]; WVB[8 + p] = f1[p]; }
      } else {
        LAS unsigned char* dst = lds + CH_LDS_WV + (w * 18 + (ks - 3) * 2) * 2048 + lane * 16;
        *(LAS v4u*)(dst) = v4u{f0[0], f0[1], f0[2], f0[3]};
        *(LAS v4u*)(dst + 1024) = v4u{f0[4], f0[5], f0[6], f0[7]};
        *(LAS v4u*)(dst + 2048) = v4u{f1[0], f1[1], f1[2], f1[3]};
        *(LAS v4u*)(dst + 3072) = v4u{f1[4], f1[5], f1[6], f1[7]};
      }
    }
    SB();
  }
  TS(1);
  {
    const rsrc_t rw = MKRS(a.Wo, DM * HD * 2);
    const int vo = ((h * DH + 8 * (g >> 1) + (g & 1)) * DM + (12 * cs + 3 * w) * 16 + n) * 2;
    u32 lw[2][8][6];
#pragma unroll
    for (int kk = 0; kk < 2; ++kk)
#pragma unroll
      for (int p = 0; p < 8; ++p) {
        const int e0 = 32 * (2 * kk + (p & 1)) + 16 * (p >> 2) + 4 * ((p >> 1) & 1);
#pragma unroll
        for (int i = 0; i < 3; ++i) {
          lw[kk][p][2 * i] = __builtin_amdgcn_raw_buffer_load_b16(rw, vo + 32 * i, e0 * DM * 2, 0); lw[kk][p][2 * i + 1] = __builtin_amdgcn_raw_buffer_load_b16(rw, vo + 32 * i, (e0 + 2) * DM * 2, 0);
        }
      }
    SB();
#pragma unroll
    for (int kk = 0; kk < 2; ++kk) {
      v8u wo[3];
#pragma unroll
      for (int p = 0; p < 8; ++p)
#pragma unroll
        for (int i = 0; i < 3; ++i) wo[i][p] = __builtin_amdgcn_perm(lw[kk][p][2 * i + 1], lw[kk][p][2 * i], 0x05040100u);
#pragma unroll
      for (int i = 0; i < 3; ++i) {
        LAS unsigned char* dst = lds + CH_LDS_WV + (w * 18 + 12 + 3 * kk + i) * 2048 + lane * 16;
        *(LAS v4u*)(dst) = v4u{wo[i][0], wo[i][1], wo[i][2], wo[i][3]};
        *(LAS v4u*)(dst + 1024) = v4u{wo[i][4], wo[i][5], wo[i][6], wo[i][7]};
      }
    }
  }
  {
#pragma unroll
    for (int j = 0; j < 3; ++j) { const int o = w * 144 + j * 64 + lane, ks = o >> 6, l = o & 63, nn = l & 15;
      const int tok = 4 * ((nn >> 2) & 1) + 2 * (nn >> 3) + ((nn >> 1) & 1);
      if (j < 2 || lane < 16) *(LAS v4u*)(lds + CH_LDS_MS + o * 16) = *(const v4u*)(a.mem0 + tok * DM + 64 * ks + 16 * (l >> 4) + 8 * (nn & 1)); }
  }
  TS(2);
  wait_ge(a.ws, FL_PAR, 0); wait_ge(a.ws, FL_PAR + 64, 0); wait_ge(a.ws, FL_PAR + 128, 0);
  const int par = __builtin_amdgcn_readfirstlane(__hip_atomic_load((GAS int*)(a.ws + FL_PAR), RLX_AGENT)) & 1;
  const int parm = (__builtin_amdgcn_readfirstlane(__hip_atomic_load((GAS int*)(a.ws + FL_PAR + 64), RLX_AGENT)) & 1) |
                   ((__builtin_amdgcn_readfirstlane(__hip_atomic_load((GAS int*)(a.ws + FL_PAR + 128), RLX_AGENT)) & 1) << 1);
  wait_ge(a.ws, FL_ARM, 63);
  TS(3);
  wait_ge(a.ws, FL_KV + h * 256, 0);
  TS(4);
  const int T0 = 12 * cs + 3 * w;
  v16u P0; v4u P1; v2u P2; u32 P3;
  const int rep = (idx >> 2) & 1, mrd = rep * 2 * MB + ((parm >> rep) & 1) * 8192;
  {
    const int o0 = w * 2304 + lane * 16;
    const int LOOB = 0x3fff0;
    P0[0] = (n & 1) ? 0xEEEE : 0x4444;
    P0[1] = lane * 16;
    P0[2] = CH_LDS_WV + w * 18 * 2048 + lane * 16;
    P0[3] = (w >> 1) * 2048 + (64 * (w & 1) + n) * 16 + (g & 1) * 8 + (g >> 1) * 4;
    P0[4] = (4 * g + (n >> 2)) * 16 + (n & 1) * 8;
    { const int na = 4 * (((n >> 2) & 1) | (n & 2)) + 2 * (n & 1) + (w & 1);
      P0[5] = n < 8 ? CH_LDS_OF + (w >> 1) * 1024 + (16 * g + na) * 16 : LOOB; }
    P0[6] = amap((T0 * 2 + th) * 4096 + h * 128 + (lane & 31) * 4, par);
    P0[7] = w * 2048 + lane * 16;
    P0[8] = o0;
    P0[9] = lane < 16 ? CH_LDS_MS + o0 + 2048 : (lane < 32 ? CH_LDS_ST + (w * 16 + lane - 16) * 16 : LOOB);
    P0[10] = o0 + mrd;
    P0[11] = o0 + 1024 + ((o0 + 1024) & 8192) + mrd;
    P0[12] = lane < 16 ? o0 + 2048 + ((o0 + 2048) & 8192) + mrd : (lane < 32 ? 16384 + 1024 + (w * 16 + lane - 16) * 64 + mrd : OOB);
    P0[13] = lane < 32 ? (w < 2 ? OFF_KX : OFF_VX) + h * 8192 + (w & 1) * 4096 + lane * 128 : OOB;
    P0[14] = (w < 2 && idx < 72) ? amap(idx * 4096, par) + lane * 16 : OOB;
    { const int c8 = lane & 7, gg = 2 * (idx & 1) + (c8 >> 2), na = 4 * gg + 2 * (g & 1) + ((c8 >> 1) & 1);
      const int o = (idx >> 1) * 256 + na * 16 + ((c8 & 1) * 2 + (g >> 1)) * 4;
      const int rw = (lane >> 3) & 1;
      P0[15] = o + (o & 8192) + rw * 2 * MB + ((parm >> rw) & 1) * 8192;
      const int rs_ = (lane >> 1) & 1;
      P3 = (lane < 4 && idx < 64) ? 16384 + 1024 + idx * 64 + (lane & 1) * 8 + rs_ * 2 * MB + ((parm >> rs_) & 1) * 8192 : OOB; }
    { const int r = lane & 3, gd = (lane >> 2) & 3, j = r < 2 ? 2 * (2 * (gd >> 1) + r) + (gd & 1) : 99; const u32 one = (j & 1) ? 0x3f800000u : 0x00003f80u;
      P1[0] = (j >> 1) == 0 ? one : 0u; P1[1] = (j >> 1) == 1 ? one : 0u; P1[2] = (j >> 1) == 2 ? one : 0u; P1[3] = (j >> 1) == 3 ? one : 0u; }
    P2[0] = (lane & 8) ? OOB : g * 128 + n * 16;
    P2[1] = lane * 8;
  }
  __syncthreads();
  const int sFL = ((w == 0 && idx < 72) ? 1 : 0) | (cs == 0 ? 2 : 0) | (w == 3 ? 4 : 0);
  u32 dd2 = a.r0, gt = (u32)OFF_DBG + (u32)MB + (idx * 4 + w) * 4096, hh = h;
  u32 f1, f2, dead; v4u st0, st1, st2;
  asm volatile(CHAIN_ASM
    : "+{s24}"(gt), "+{s17}"(dd2), "+{s12}"(hh), "+{v[0:15]}"(P0), "={s31}"(f1), "={s32}"(f2), "={s33}"(dead), "={s[40:43]}"(st0), "={s[44:47]}"(st1), "={s[48:51]}"(st2)
    : "{a[0:31]}"(wqk[0]), "{a[32:63]}"(wqk[1]), "{a[64:95]}"(wqk[2]), "{a[96:127]}"(wqk[3]), "{a[128:159]}"(wqk[4]), "{a[160:191]}"(wqk[5]),
      "{a[192:223]}"(wqk[6]), "{a[224:255]}"(wqk[7]), "{v[176:207]}"(wqk[8]), "{v[208:239]}"(WVA), "{v[240:255]}"(WVB),
      "{v[16:19]}"(P1), "{v[20:21]}"(P2), "{v22}"(P3), "{s35}"(w * 512), "{s[8:11]}"(rs), "{s16}"(a.d1), "{s18}"(sFL), "{s39}"(a.delta), "{s74}"(a.a0), "{s77}"(OFF_P0)
    : CHAIN_CLOBBERS);
}

template <int NK> __device__ __forceinline__ void load_wpairs(rsrc_t rw, int vo, int ks0, v4u (*f)[2]) {
  u32 l[NK][8];
#pragma unroll
  for (int k = 0; k < NK; ++k)
#pragma unroll
    for (int p = 0; p < 8; ++p) l[k][p] = __builtin_amdgcn_raw_buffer_load_b32(rw, vo + (ks0 + k) * 32 * HD * 2, p * HD * 2, 0);
  SB();
#pragma unroll
  for (int k = 0; k < NK; ++k)
#pragma unroll
    for (int p = 0; p < 4; ++p) {
      f[ks0 + k][0][p] = __builtin_amdgcn_perm(l[k][2 * p + 1], l[k][2 * p], 0x05040100u);
      f[ks0 + k][1][p] = __builtin_amdgcn_perm(l[k][2 * p + 1], l[k][2 * p], 0x07060302u);
    }
}
constexpr int XRS = 1168, XBUF = 32 * XRS;
__device__ __forceinline__ v4u x_load(rsrc_t rx, int s, int w, int lane, int i) { return __builtin_amdgcn_raw_buffer_load_b128(rx, lane * 16 + i * 1024, s * 36864 + w * 9216, 0); }
__device__ __forceinline__ int x_dst(int w, int lane, int i) { const int q = (9 * w + i) * 64 + lane, r = q / 72; return r * XRS + (q - 72 * r) * 16; }
#define MFA(c, a, b) asm("v_mfma_f32_16x16x32_bf16 %0, %1, %2, %0" : "+v"(c) : "a"(a), "v"(b))
#define MFB(c, a, b) asm("v_mfma_f32_16x16x32_bf16 %0, %1, %2, %0" : "+v"(c) : "v"(a), "v"(b))
#define MFC(c, a, b) asm("v_mfma_f32_16x16x32_bf16 %0, %1, %2, %0" : "+v"(c) : "v"(a), "a"(b))
__device__ __forceinline__ v4u pack8(f32x4 a, f32x4 b) { return v4u{pk2(a[0], a[1]), pk2(a[2], a[3]), pk2(b[0], b[1]), pk2(b[2], b[3])}; }

__device__ __forceinline__ void kv_role(const Args& a, LAS unsigned char* lds, int w, int h) {
  const int lane = lane_id(), n = lane & 15, g = lane >> 4;
  const rsrc_t rs = MKRS(a.ws, 0x7fffffff), rx = MKRS(a.x, 2048 * DM * 2);
  v4u xr[9]; int xd[9];
#pragma unroll
  for (int i = 0; i < 9; ++i) { xr[i] = x_load(rx, 0, w, lane, i); xd[i] = x_dst(w, lane, i); }
  v4u wk[18][2], wv[18][2];
  {
    const rsrc_t rk = MKRS(a.Wk, DM * HD * 2), rv = MKRS(a.Wv, DM * HD * 2);
    const int vk = (8 * g * HD + h * DH + 64 * (w >> 1) + 4 * n + 2 * (w & 1)) * 2, vv = (8 * g * HD + h * DH + 32 * w + 2 * n) * 2;
    u32 l[2][72];
#define WISSUE(set, rw, vo, ks0) do { _Pragma("unroll") for (int k = 0; k < 9; ++k) _Pragma("unroll") for (int p = 0; p < 8; ++p) \
      l[set][8 * k + p] = __builtin_amdgcn_raw_buffer_load_b32(rw, vo + ((ks0) + k) * 32 * HD * 2, p * HD * 2, 0); SB(); } while (0)
#define WPACK(set, f, ks0, NA) do { _Pragma("unroll") for (int k = 0; k < 9; ++k) { _Pragma("unroll") for (int p = 0; p < 4; ++p) { \
      f[(ks0) + k][0][p] = __builtin_amdgcn_perm(l[set][8 * k + 2 * p + 1], l[set][8 * k + 2 * p], 0x05040100u); \
      f[(ks0) + k][1][p] = __builtin_amdgcn_perm(l[set][8 * k + 2 * p + 1], l[set][8 * k + 2 * p], 0x07060302u); } \
      if ((ks0) + k < (NA)) asm volatile("" : "+a"(f[(ks0) + k][0]), "+a"(f[(ks0) + k][1])); else asm volatile("" : "+v"(f[(ks0) + k][0]), "+v"(f[(ks0) + k][1])); } SB(); } while (0)
    WISSUE(0, rk, vk, 0); WISSUE(1, rk, vk, 9);
    WPACK(0, wk, 0, 18); WISSUE(0, rv, vv, 0);
    WPACK(1, wk, 9, 18); WISSUE(1, rv, vv, 9);
    WPACK(0, wv, 0, 14); WPACK(1, wv, 9, 14);
  }
#pragma unroll
  for (int i = 0; i < 9; ++i) *(LAS v4u*)(lds + xd[i]) = xr[i];
  __syncthreads();
  TS(0);
  for (int s = 0; s < NSEG; ++s) {
    const LAS unsigned char* cur = lds + (s & 1) * XBUF + n * XRS + g * 16;
    LAS unsigned char* nxt = lds + ((s + 1) & 1) * XBUF;
    const int sn = s + 1 < NSEG ? s + 1 : s;
    f32x4 ak[2][2], av[2][2];
#pragma unroll
    for (int i = 0; i < 4; ++i) { ak[i >> 1][i & 1] = f32x4{0.f, 0.f, 0.f, 0.f}; av[i >> 1][i & 1] = f32x4{0.f, 0.f, 0.f, 0.f}; }
    asm volatile("s_nop 1" : "+v"(ak[0][0]), "+v"(ak[0][1]), "+v"(ak[1][0]), "+v"(ak[1][1]), "+v"(av[0][0]), "+v"(av[0][1]), "+v"(av[1][0]), "+v"(av[1][1]));
    v4u xf[2][6];
#define XRD(kb) do { _Pragma("unroll") for (int i = 0; i < 6; ++i) xf[(kb) & 1][i] = *(const LAS v4u*)(cur + (i & 1) * 16 * XRS + ((kb) * 3 + (i >> 1)) * 64); } while (0)
    XRD(0);
#pragma unroll
    for (int kb = 0; kb < 6; ++kb) {
#pragma unroll
      for (int i = 0; i < 6; ++i) { const int ks = kb * 3 + (i >> 1), tt = i & 1;
        SB();
        MFA(ak[tt][0], wk[ks][0], xf[kb & 1][i]);
        SB();
        if (kb < 5) xf[(kb + 1) & 1][i] = *(const LAS v4u*)(cur + (i & 1) * 16 * XRS + ((kb + 1) * 3 + (i >> 1)) * 64);
        SB();
        MFA(ak[tt][1], wk[ks][1], xf[kb & 1][i]);
        SB();
        if (i & 1) { if (kb < 3) xr[3 * kb + (i >> 1)] = x_load(rx, sn, w, lane, 3 * kb + (i >> 1)); else *(LAS v4u*)(nxt + xd[3 * (kb - 3) + (i >> 1)]) = xr[3 * (kb - 3) + (i >> 1)]; }
        SB();
        if (ks < 14) { MFC(av[0][tt], xf[kb & 1][i], wv[ks][0]); MFC(av[1][tt], xf[kb & 1][i], wv[ks][1]); }
        else { MFB(av[0][tt], xf[kb & 1][i], wv[ks][0]); MFB(av[1][tt], xf[kb & 1][i], wv[ks][1]); } }
    }
    SB();
    asm volatile("s_nop 7" : "+v"(ak[0][0]), "+v"(ak[0][1]), "+v"(ak[1][0]), "+v"(ak[1][1]), "+v"(av[0][0]), "+v"(av[0][1]), "+v"(av[1][0]), "+v"(av[1][1]));
    publish_set(a.ws, FL_KV + h * 256, s - 1, w == 0 && lane == 0);
    const int sb = (s * 32 + h) * 8192;
    st128(pack8(ak[0][0], ak[0][1]), rs, lane * 16 + w * 1024, OFF_KX + sb);
    st128(pack8(ak[1][0], ak[1][1]), rs, lane * 16 + w * 1024 + 4096, OFF_KX + sb);
    st128(pack8(av[0][0], av[0][1]), rs, lane * 16 + w * 2048, OFF_VX + sb);
    st128(pack8(av[1][0], av[1][1]), rs, lane * 16 + w * 2048 + 1024, OFF_VX + sb);
    st128(v4u{SENT, SENT, SENT, SENT}, rs, lane * 16 + (w & 1) * 1024, (w < 2 ? OFF_KM : OFF_VM) + (s * 32 + h) * 2048);
    if (s < 3 || s == NSEG - 1) publish_set(a.ws, FL_KV + h * 256, s, w == 0 && lane == 0);
    if (s < 4) TS(1 + s);
  }
}

__device__ __forceinline__ void qa_role(const Args& a, LAS unsigned char* lds, int w, int h, int par) {
  const int lane = lane_id(), n = lane & 15, g = lane >> 4;
  const rsrc_t rs = MKRS(a.ws, 0x7fffffff), rx = MKRS(a.x, 2048 * DM * 2);
  for (int i = 0; i < 24; ++i) __builtin_amdgcn_s_sleep(8);
  for (int i = 0; i < 1; ++i) __builtin_amdgcn_s_sleep(127);
  v4u xr[9]; int xd[9];
#pragma unroll
  for (int i = 0; i < 9; ++i) { xr[i] = x_load(rx, par, w, lane, i); xd[i] = x_dst(w, lane, i); }
  v4u wq[18][2];
  {
    const rsrc_t rq = MKRS(a.Wq, DM * HD * 2);
    const int vq = (8 * g * HD + h * DH + 64 * (w >> 1) + 4 * n + 2 * (w & 1)) * 2;
    load_wpairs<9>(rq, vq, 0, wq); SB(); load_wpairs<9>(rq, vq, 9, wq);
  }
#pragma unroll
  for (int i = 0; i < 9; ++i) *(LAS v4u*)(lds + xd[i]) = xr[i];
  __syncthreads();
  TS(0);
  LAS unsigned char* qx = lds + 2 * XBUF;
  const int qt = w & 1, eh = w >> 1;
  const float cexp = 0.08838834764831845f * 1.4426950408889634f;
  const bool lo = lane < 32;
  for (int s = par; s < NSEG; s += 2) {
    const LAS unsigned char* cur = lds + ((s >> 1) & 1) * XBUF + n * XRS + g * 16;
    LAS unsigned char* nxt = lds + (((s >> 1) + 1) & 1) * XBUF;
    const int sn = s + 2 < NSEG ? s + 2 : s;
    wait_ge(a.ws, FL_KV + h * 256, s);
    const int sb = (s * 32 + h) * 8192, mb = (s * 32 + h) * 2048;
    v4u k0[4], k1[4], vx[4];
    f32x4 aq[2][2];
#pragma unroll
    for (int i = 0; i < 4; ++i) aq[i >> 1][i & 1] = f32x4{0.f, 0.f, 0.f, 0.f};
    v4u xf[2][6];
    XRD(0);
#pragma unroll
    for (int kb = 0; kb < 6; ++kb) {
      SB();
      if (kb < 5) XRD(kb + 1);
      if (kb < 3) {
#pragma unroll
        for (int i = 0; i < 3; ++i) xr[3 * kb + i] = x_load(rx, sn, w, lane, 3 * kb + i);
      }
      if (kb >= 2) { const int kq = kb - 2;
        k0[kq] = ld128(rs, lane * 16 + kq * 1024, OFF_KX + sb); k1[kq] = ld128(rs, qt ? lane * 16 + kq * 1024 : OOB, OFF_KX + sb + 4096);
        vx[kq] = ld128(rs, lane * 16 + kq * 1024, OFF_VX + sb + eh * 4096); }
#pragma unroll
      for (int i = 0; i < 6; ++i) { const int ks = kb * 3 + (i >> 1), tt = i & 1;
        aq[tt][0] = mfma32(wq[ks][0], xf[kb & 1][i], aq[tt][0]); aq[tt][1] = mfma32(wq[ks][1], xf[kb & 1][i], aq[tt][1]); }
#pragma unroll
      for (int u = 0; u < 6; ++u) { SGB(0x8, 1); SGB(0x100, 1); SGB(0x8, 1); SGB(0x20, 1); }
    }
    SB();
    *(LAS v4u*)(qx + w * 1024 + lane * 16) = pack8(aq[0][0], aq[0][1]);
    *(LAS v4u*)(qx + (4 + w) * 1024 + lane * 16) = pack8(aq[1][0], aq[1][1]);
#pragma unroll
    for (int i = 0; i < 9; ++i) *(LAS v4u*)(nxt + xd[i]) = xr[i];
    if (s == 40) TS(1);
    __syncthreads();
    v4u qf[4];
#pragma unroll
    for (int kq = 0; kq < 4; ++kq) qf[kq] = *(const LAS v4u*)(qx + (qt * 4 + kq) * 1024 + lane * 16);
    f32x4 s1 = {0.f, 0.f, 0.f, 0.f}, s2 = s1, s0 = s1;
#pragma unroll
    for (int kq = 0; kq < 4; ++kq) { s1 = mfma32(k0[kq], qf[kq], s1); s2 = mfma32(k1[kq], qf[kq], s2); }
    if (s == 40) TS(3);
    v4u km[4]; v2u vm[2];
    {
      GAS u32* p = (GAS u32*)(a.ws + OFF_KM + mb + w * 512 + 508);
      for (int sp = 0; sp < SPIN_CAP; ++sp) {
        if (__builtin_amdgcn_readfirstlane(__hip_atomic_load(p, RLX_AGENT)) != SENT) break;
        if ((sp & 255) == 255 && __builtin_amdgcn_readfirstlane(__hip_atomic_load((GAS int*)(a.ws + FL_ERR), RLX_AGENT)) != -1) break;
        __builtin_amdgcn_s_sleep(8);
      }
    }
    for (int sp = 0;; ++sp) {
      asm volatile("" ::: "memory");
#pragma unroll
      for (int kq = 0; kq < 4; ++kq) km[kq] = ld128(rs, g * 128 + (n & 7) * 16 + kq * 512, OFF_KM + mb);
      vm[0] = __builtin_amdgcn_raw_buffer_load_b64(rs, lane * 8, OFF_VM + mb + eh * 1024, SC1);
      vm[1] = __builtin_amdgcn_raw_buffer_load_b64(rs, lane * 8 + 512, OFF_VM + mb + eh * 1024, SC1);
      u32 mx = max(max(vm[0].x, vm[0].y), max(vm[1].x, vm[1].y));
#pragma unroll
      for (int kq = 0; kq < 4; ++kq) mx = max(max(mx, max(km[kq].x, km[kq].y)), max(km[kq].z, km[kq].w));
      if (!__any(mx == SENT)) break;
      if (sp > SPIN_CAP) { __hip_atomic_store((GAS int*)(a.ws + FL_ERR), 1, RLX_AGENT); break; }
      if ((sp & 255) == 255 && __builtin_amdgcn_readfirstlane(__hip_atomic_load((GAS int*)(a.ws + FL_ERR), RLX_AGENT)) != -1) break;
      __builtin_amdgcn_s_sleep(4);
    }
#pragma unroll
    for (int kq = 0; kq < 4; ++kq) s0 = mfma32(km[kq], qf[kq], s0);
    if (s == 40) TS(4);
    const int q = 16 * qt + n;
    float t[10];
#pragma unroll
    for (int r = 0; r < 4; ++r) { t[r] = (4 * g + r <= q) ? s1[r] : -3.0e38f; t[4 + r] = (16 + 4 * g + r <= q) ? s2[r] : -3.0e38f; }
    t[8] = lo ? s0[0] : s0[2]; t[9] = lo ? s0[1] : s0[3];
    float m = t[0];
#pragma unroll
    for (int i = 1; i < 10; ++i) m = fmaxf(m, t[i]);
    m = xmax(m);
    const float mc = m * cexp;
    float l = 0.f;
#pragma unroll
    for (int i = 0; i < 10; ++i) { t[i] = __builtin_amdgcn_exp2f(t[i] * cexp - mc); }
    const v4u pb = {pk2(t[0], t[1]), pk2(t[2], t[3]), pk2(t[4], t[5]), pk2(t[6], t[7])};
    const u32 pa = pk2(t[8], t[9]);
    l = ((bflo(pb.x) + bfhi(pb.x)) + (bflo(pb.y) + bfhi(pb.y))) + ((bflo(pb.z) + bfhi(pb.z)) + (bflo(pb.w) + bfhi(pb.w))) + (bflo(pa) + bfhi(pa));
    const float rl = __builtin_amdgcn_rcpf(xsum(l));
#pragma unroll
    for (int i = 0; i < 2; ++i) {
      f32x4 o0 = mfma32(vx[2 * i], pb, f32x4{0.f, 0.f, 0.f, 0.f}), o1 = mfma32(vx[2 * i + 1], pb, f32x4{0.f, 0.f, 0.f, 0.f});
      o0 = mfma16(lo ? v2u{vm[i].x, 0u} : v2u{0u, vm[i].x}, v2u{pa, pa}, o0);
      o1 = mfma16(lo ? v2u{vm[i].y, 0u} : v2u{0u, vm[i].y}, v2u{pa, pa}, o1);
      st128(pack8(o0 * rl, o1 * rl), rs, lane * 16 + (qt * 4 + 2 * eh + i) * 1024, OFF_O + sb);
    }
    if (s == 40) TS(5);
    publish_set(a.ws, FL_O + h * 256 + par * 128, s, w == 0 && lane == 0);
    if (s == 38) TS(6);
    if (s == 40) TS(7);
  }
}

__device__ __forceinline__ float phi(float t) { return (t > 0.f ? t + 1.f : __expf(t)) + 1e-6f; }

__device__ __forceinline__ void bo_role(const Args& a, LAS unsigned char* lds, int w, int id) {
  const int lane = lane_id(), tid = w * 64 + lane, n = lane & 15, g = lane >> 4;
  const rsrc_t rs = MKRS(a.ws, 0x7fffffff), rx = MKRS(a.x, 2048 * DM * 2);
  {
    const int ho = ho_off(a.ws);
    if (id == 48 || id == 52 || id == 56) {
      if (tid == 0) {
        unsigned char* p = a.ws + ho + (id == 48 ? 0x1F0000 : (id - 48) * (MB / 2) + 16384 + 6144);
        u32 l0 = ~0u, l1 = ~0u;
#pragma unroll 1
        for (int r = 0; r < 5; ++r) { l0 = min(l0, probe_once(p + r * 256)); l1 = min(l1, probe_once(p + 8192 + r * 256)); }
        __hip_atomic_store((GAS int*)(a.ws + FL_PAR + (id - 48) * 16), (int)((l1 < l0 ? 1 : 0) | (l0 << 4) | (l1 << 18)) & 0x7fffffff, RLX_AGENT);
      }
      __syncthreads();
    }
    for (int i = id * 256 + tid; i < 6 * MB / 16; i += 64 * 256) st128(v4u{SENT, SENT, SENT, SENT}, rs, i * 16, ho);
    publish_add(a.ws, FL_ARM, tid == 0);
  }
  TS(0);
  const int kg = id & 3, cg = id < 48 ? id >> 2 : 0, h0 = 8 * kg + 2 * w;
  v4u wf[8][3];
  {
    const int tb = id >> 2, hg = id & 3;
    constexpr int RS = 27 * 32 + 16, TR = 272;
    LAS unsigned char* T = lds + 16384;
    {
      const rsrc_t rb = MKRS(a.wqkv, DM * 1728 * 2);
      f32x4 acc[2][27];
#pragma unroll
      for (int i = 0; i < 2; ++i)
#pragma unroll
        for (int j = 0; j < 27; ++j) acc[i][j] = f32x4{0.f, 0.f, 0.f, 0.f};
      int bo[14], lo[14];
#pragma unroll
      for (int i = 0; i < 14; ++i) { const int c = tid + 256 * i, r = c / 54, cc = c - r * 54, p = cc / 18;
        bo[i] = c < 64 * 54 ? (r * 1728 + p * 576 + hg * 144 + (cc - 18 * p) * 8) * 2 : OOB; lo[i] = c < 64 * 54 ? r * RS + cc * 16 : 0x3fff0; }
      const int ao = ((tb * 128 + w * 32 + n) * DM + 8 * g) * 2;
      v4u br[2][14], ar[2][4];
      auto fetch1 = [&](int set, int k0, int i) {
        if (i < 14) br[set][i] = __builtin_amdgcn_raw_buffer_load_b128(rb, bo[i], k0 * 1728 * 2, 0);
        else ar[set][i - 14] = __builtin_amdgcn_raw_buffer_load_b128(rx, ao + ((i - 14) >> 1) * 16 * DM * 2 + (i & 1) * 64, k0 * 2, 0);
      };
#pragma unroll
      for (int i = 0; i < 18; ++i) fetch1(0, 0, i);
#pragma unroll
      for (int i = 0; i < 18; ++i) fetch1(1, 64, i);
#pragma unroll 1
      for (int sp = 0; sp < 5; ++sp)
#pragma unroll
      for (int set = 0; set < 2; ++set) {
        const int st = 2 * sp + set;
        if (st == 9) break;
        LAS unsigned char* buf = lds + set * (64 * RS);
#pragma unroll
        for (int i = 0; i < 14; ++i) *(LAS v4u*)((LAS unsigned char*)(uintptr_t)0 + ((u32)(uintptr_t)buf + lo[i])) = br[set][i];
        v4u af[4];
#pragma unroll
        for (int i = 0; i < 4; ++i) af[i] = ar[set][i];
        __syncthreads();
        const int kn = st < 7 ? 64 * (st + 2) : 512;
        const LAS unsigned char* p = buf + (8 * g + (n >> 2)) * RS + (n & 3) * 8;
        v4u bq[2][6];
#define BRD(q) do { _Pragma("unroll") for (int u = 0; u < 6; ++u) { const int z = 6 * (q) + u, kh = z / 27, j = z - 27 * kh; \
          const v2u b0 = trread(p + kh * 32 * RS + j * 32), b1 = trread(p + kh * 32 * RS + j * 32 + 4 * RS); bq[(q) & 1][u] = v4u{b0.x, b0.y, b1.x, b1.y}; } } while (0)
        BRD(0);
#pragma unroll
        for (int q = 0; q < 9; ++q) {
          SB();
          if (q < 8) BRD(q + 1);
          if (q < 6) { fetch1(set, kn, 3 * q); fetch1(set, kn, 3 * q + 1); fetch1(set, kn, 3 * q + 2); }
#pragma unroll
          for (int u = 0; u < 6; ++u) { const int z = 6 * q + u, kh = z / 27, j = z - 27 * kh;
            acc[0][j] = mfma32(af[kh], bq[q & 1][u], acc[0][j]); acc[1][j] = mfma32(af[2 + kh], bq[q & 1][u], acc[1][j]); }
#pragma unroll
          for (int u = 0; u < 12; ++u) { SGB(0x8, 1); SGB(0x100, 1); if ((u & 3) == 3) SGB(0x20, 1); }
        }
        SB();
      }
      __syncthreads();
      TS(11);
#pragma unroll
      for (int i = 0; i < 2; ++i)
#pragma unroll
        for (int j = 0; j < 27; ++j) {
          f32x4 v = acc[i][j];
          if (j < 18) { v[0] = phi(v[0]); v[1] = phi(v[1]); v[2] = phi(v[2]); v[3] = phi(v[3]); }
          *(LAS v2u*)(T + (j * 16 + n) * TR + (w * 32 + i * 16 + 4 * g) * 2) = v2u{pk2(v[0], v[1]), pk2(v[2], v[3])};
        }
      if (tid < 128) *(LAS u32*)(T + (432 + (tid >> 6)) * TR + (tid & 63) * 4) = tid < 64 ? 0x3f803f80u : 0u;
    }
    __syncthreads();
    TS(1);
#pragma unroll
    for (int i = 0; i < 2; ++i) {
      const int hh = 2 * w + i;
      const LAS unsigned char* ka = T + (144 + hh * 18 + n) * TR + g * 16;
      const LAS unsigned char* kb = T + (n < 2 ? 160 + hh * 18 + n : 433) * TR + g * 16;
      const LAS unsigned char* va = T + (288 + hh * 18 + n) * TR + g * 16;
      const LAS unsigned char* vb = T + (n < 2 ? 304 + hh * 18 + n : (n == 2 ? 432 : 433)) * TR + g * 16;
      f32x4 c00 = {0.f, 0.f, 0.f, 0.f}, c01 = c00, c10 = c00, c11 = c00;
#pragma unroll
      for (int ks = 0; ks < 4; ++ks) {
        const v4u k0 = *(const LAS v4u*)(ka + ks * 64), k1 = *(const LAS v4u*)(kb + ks * 64), v0 = *(const LAS v4u*)(va + ks * 64), v1 = *(const LAS v4u*)(vb + ks * 64);
        c00 = mfma32(k0, v0, c00); c01 = mfma32(k0, v1, c01); c10 = mfma32(k1, v0, c10); c11 = mfma32(k1, v1, c11);
      }
      const int base = OFF_KVP + (tb * 32 + hg * 8 + hh) * 1600;
      st128(__builtin_bit_cast(v4u, c00), rs, n * 80 + g * 16, base);
      st128(__builtin_bit_cast(v4u, c10), rs, g == 0 ? n * 80 + 64 : OOB, base);
      st128(__builtin_bit_cast(v4u, c01), rs, n < 4 ? (16 + n) * 80 + g * 16 : OOB, base);
      st128(__builtin_bit_cast(v4u, c11), rs, (n < 4 && g == 0) ? (16 + n) * 80 + 64 : OOB, base);
    }
    publish_add(a.ws, FL_B1, tid == 0);
    TS(2);
  {
    const rsrc_t rw = MKRS(a.Wo, DM * HD * 2);
    const int vo = ((h0 * DH + 8 * g) * DM + 48 * cg + n) * 2;
#pragma unroll
    for (int fh = 0; fh < 2; ++fh) {
      u32 lw[4][4][6];
#pragma unroll
      for (int f = 0; f < 4; ++f)
#pragma unroll
        for (int p = 0; p < 4; ++p)
#pragma unroll
          for (int i = 0; i < 3; ++i) {
            const int e0 = 32 * (4 * fh + f) + 4 * (p & 1) + (p >> 1);
            lw[f][p][2 * i] = __builtin_amdgcn_raw_buffer_load_b16(rw, vo + 32 * i, e0 * DM * 2, 0); lw[f][p][2 * i + 1] = __builtin_amdgcn_raw_buffer_load_b16(rw, vo + 32 * i, (e0 + 2) * DM * 2, 0);
          }
      SB();
#pragma unroll
      for (int f = 0; f < 4; ++f)
#pragma unroll
        for (int p = 0; p < 4; ++p)
#pragma unroll
          for (int i = 0; i < 3; ++i) wf[4 * fh + f][i][p] = __builtin_amdgcn_perm(lw[f][p][2 * i + 1], lw[f][p][2 * i], 0x05040100u);
#pragma unroll
      for (int f = 0; f < 4; ++f) asm volatile("" : "+v"(wf[4 * fh + f][0]), "+v"(wf[4 * fh + f][1]), "+v"(wf[4 * fh + f][2]));
      SB();
    }
  }
    if (w == 0) wait_ge(a.ws, FL_B1, 63);
    __syncthreads();
    TS(3);
    for (int it = tid; it < 800; it += 256) {
      v4u pv[16];
#pragma unroll
      for (int c = 0; c < 16; ++c) pv[c] = ld128(rs, it * 16, OFF_KVP + (c * 32 + hg * 8) * 1600);
      SB();
      f32x4 sum = {0.f, 0.f, 0.f, 0.f};
#pragma unroll
      for (int c = 0; c < 16; ++c) sum += __builtin_bit_cast(f32x4, pv[c]);
      const int row = it / 5, d4 = it - 5 * row;
      *(LAS v2u*)(lds + row * 64 + d4 * 8) = v2u{pk2(sum[0], sum[1]), pk2(sum[2], sum[3])};
      if (d4 < 3) *(LAS v2u*)(lds + row * 64 + 40 + d4 * 8) = v2u{0u, 0u};
    }
    __syncthreads();
    TS(12);
#pragma unroll
    for (int i = 0; i < 2; ++i) {
      const int hh = 2 * w + i;
      const v4u e0 = *(const LAS v4u*)(lds + (hh * 20 + n) * 64 + g * 16), e1 = *(const LAS v4u*)(lds + (hh * 20 + 16 + (n & 3)) * 64 + g * 16);
      const int d0 = 8 * g + (n >> 2);
      auto obo = [&](int c) { return (c >> 5) * 1024 + (((c >> 3) & 3) * 16 + n) * 16 + (c & 7) * 2; };
      const int c0 = hg * 144 + hh * 18, ob0 = obo(c0 + 4 * g), ob1 = obo(c0 + 4 * g + 2), ob2 = g == 0 ? obo(c0 + 16) : OOB;
      const LAS unsigned char* q0 = T + (d0 < 18 ? hh * 18 + d0 : 433) * TR + (n & 3) * 8;
      const LAS unsigned char* q1 = T + (d0 + 4 < 18 ? hh * 18 + d0 + 4 : 433) * TR + (n & 3) * 8;
#pragma unroll
      for (int tt = 0; tt < 8; ++tt) {
        const v2u x0 = trread(q0 + tt * 32), x1 = trread(q1 + tt * 32);
        const v4u qf = {x0.x, x0.y, x1.x, x1.y};
        const f32x4 n0 = mfma32(e0, qf, f32x4{0.f, 0.f, 0.f, 0.f}), n1 = mfma32(e1, qf, f32x4{0.f, 0.f, 0.f, 0.f});
        const float rd = 1.f / (n1[2] + 1e-6f);
        const int so = OFF_OB + (tb * 8 + tt) * 18432;
        __builtin_amdgcn_raw_buffer_store_b32(pk2(n0[0] * rd, n0[1] * rd), rs, ob0, so, SC1);
        __builtin_amdgcn_raw_buffer_store_b32(pk2(n0[2] * rd, n0[3] * rd), rs, ob1, so, SC1);
        __builtin_amdgcn_raw_buffer_store_b32(pk2(n1[0] * rd, n1[1] * rd), rs, ob2, so, SC1);
      }
    }
    TS(13);
    {
      const int t = id * 32 + (tid >> 3), q8 = tid & 7;
      float sum = 0.f;
#pragma unroll
      for (int d = 0; d < 72; d += 8) {
        const v4u xv = *(const v4u*)(a.x + (size_t)t * DM + q8 * 72 + d), gv = *(const v4u*)(a.gw + q8 * 72 + d);
#pragma unroll
        for (int i = 0; i < 4; ++i) sum += rbf(bflo(xv[i]) * bflo(gv[i])) + rbf(bfhi(xv[i]) * bfhi(gv[i]));
      }
      sum += __shfl_xor(sum, 1); sum += __shfl_xor(sum, 2); sum += __shfl_xor(sum, 4);
      if (q8 == 0) __builtin_amdgcn_raw_buffer_store_b32(__float_as_uint(rbf(1.f / (1.f + __expf(-rbf(sum))))), rs, t * 4, OFF_G, SC1);
    }
    publish_add(a.ws, FL_B2, tid == 0);
  }
  TS(4);
  if (id >= 60) return;
  if (id >= 48) {
    const int cb = id - 48;
    v4u wb[18][3];
    {
      const rsrc_t rw = MKRS(a.wo, DM * DM * 2);
      const int vo = (8 * g * DM + 48 * cb + n) * 2;
#pragma unroll
      for (int kh = 0; kh < 6; ++kh) {
        u32 lw[3][8][3];
#pragma unroll
        for (int k = 0; k < 3; ++k)
#pragma unroll
          for (int j = 0; j < 8; ++j)
#pragma unroll
            for (int i = 0; i < 3; ++i) lw[k][j][i] = __builtin_amdgcn_raw_buffer_load_b16(rw, vo + 32 * i, (32 * (3 * kh + k) + j) * DM * 2, 0);
        SB();
#pragma unroll
        for (int k = 0; k < 3; ++k) {
#pragma unroll
          for (int i = 0; i < 3; ++i)
#pragma unroll
            for (int p = 0; p < 4; ++p) wb[3 * kh + k][i][p] = __builtin_amdgcn_perm(lw[k][2 * p + 1][i], lw[k][2 * p][i], 0x05040100u);
          asm volatile("" : "+v"(wb[3 * kh + k][0]), "+v"(wb[3 * kh + k][1]), "+v"(wb[3 * kh + k][2]));
        }
        SB();
      }
    }
    wait_ge(a.ws, FL_B2, 63);
    const int ao = w * 18432 + lane * 16;
    v4u af[18];
#pragma unroll
    for (int ks = 0; ks < 18; ++ks) af[ks] = ld128(rs, ao + (ks & 3) * 1024, OFF_OB + (ks >> 2) * 4096);
#pragma unroll 1
    for (int blk = 0; blk < NSEG / 2; ++blk) {
      const int t = blk * 64 + w * 16 + n, nb = blk + 1 < NSEG / 2 ? blk + 1 : blk;
      const float og = 1.f - __uint_as_float(__builtin_amdgcn_raw_buffer_load_b32(rs, t * 4, OFF_G, SC1));
      f32x4 acc[3] = {f32x4{0.f, 0.f, 0.f, 0.f}, f32x4{0.f, 0.f, 0.f, 0.f}, f32x4{0.f, 0.f, 0.f, 0.f}};
#pragma unroll
      for (int ks = 0; ks < 18; ++ks) {
        SB();
#pragma unroll
        for (int i = 0; i < 3; ++i) acc[i] = mfma32(wb[ks][i], af[ks], acc[i]);
        SB();
        af[ks] = ld128(rs, ao + (ks & 3) * 1024, OFF_OB + nb * 73728 + (ks >> 2) * 4096);
      }
      SB();
      if (blk > 0) { asm volatile("s_waitcnt vmcnt(18)" ::: "memory"); __syncthreads(); if (tid == 0) __hip_atomic_store((GAS int*)(a.ws + FL_BT + cb * 64), blk - 1, RLX_AGENT); }
#pragma unroll
      for (int i = 0; i < 3; ++i) st128(__builtin_bit_cast(v4u, acc[i] * og), rs, (t * DM + 48 * cb + i * 16 + 4 * g) * 4, OFF_BT);
    }
    publish_set(a.ws, FL_BT + cb * 64, NSEG / 2 - 1, tid == 0);
    return;
  }
  TS(6);
  TS(7);
  LAS f32x4* red = (LAS f32x4*)lds;
  GAS int* fp = (GAS int*)(a.ws + FL_O + h0 * 256 + (lane & 3) * 128);
  v4u of[4][8];
  auto issue = [&](int s, int hf) {
    __builtin_amdgcn_fence(__ATOMIC_ACQUIRE, "wavefront");
#pragma unroll
    for (int f = 0; f < 8; ++f)
#pragma unroll
      for (int tt = 0; tt < 2; ++tt) {
        const v4u v = ld128(rs, lane * 16 + (tt * 4 + (f & 3)) * 1024, OFF_O + ((s + hf) * 32 + h0 + (f >> 2)) * 8192);
        if (hf == 0) of[tt][f] = v; else of[2 + tt][f] = v;
      }
    SB();
  };
  int bt_seen = -1;
  auto combine = [&](int blk) {
    if (w < 3) {
      if (bt_seen < blk) { wait_ge(a.ws, FL_BT + cg * 64, blk); bt_seen = __builtin_amdgcn_readfirstlane(__hip_atomic_load((GAS int*)(a.ws + FL_BT + cg * 64), RLX_AGENT)); }
      const int t = blk * 64 + kg * 16 + n, c = 48 * cg + w * 16 + 4 * g;
      const f32x4 b4 = __builtin_bit_cast(f32x4, ld128(rs, (t * DM + c) * 4, OFF_BT));
      const float gv = __uint_as_float(__builtin_amdgcn_raw_buffer_load_b32(rs, t * 4, OFF_G, SC1));
      wait_ge(a.ws, FL_TK + (blk * 12 + cg) * 64, 3);
      const int slab = OFF_SLAB + ((blk * 12 + cg) * 4) * 12288, it = (kg * 3 + w) * 64 + lane;
      v4u pp[4];
#pragma unroll
      for (int q = 0; q < 4; ++q) pp[q] = ld128(rs, it * 16, slab + q * 12288);
      const f32x4 av = (__builtin_bit_cast(f32x4, pp[0]) + __builtin_bit_cast(f32x4, pp[1])) + (__builtin_bit_cast(f32x4, pp[2]) + __builtin_bit_cast(f32x4, pp[3]));
      *(v2u*)(a.out + (size_t)t * DM + c) = v2u{pk2(gv * av[0] + b4[0], gv * av[1] + b4[1]), pk2(gv * av[2] + b4[2], gv * av[3] + b4[3])};
    }
  };
  bool have[2] = {false, false};
  int pend = -1, nf = -1;
#pragma unroll 1
  for (int blk = 0; blk < NSEG / 2; ++blk) {
    const int s = 2 * blk;
#pragma unroll
    for (int hf = 0; hf < 2; ++hf) {
      if (!have[hf]) {
        for (int sp = 0;; ++sp) {
          nf = __hip_atomic_load(fp, RLX_AGENT);
          if (__all((lane & 1) != hf || nf >= s + hf)) break;
          if (pend >= 0) { combine(pend); pend = -1; continue; }
          if (sp > SPIN_CAP) { __hip_atomic_store((GAS int*)(a.ws + FL_ERR), 2, RLX_AGENT); break; }
          if ((sp & 255) == 255 && __builtin_amdgcn_readfirstlane(__hip_atomic_load((GAS int*)(a.ws + FL_ERR), RLX_AGENT)) != -1) break;
          __builtin_amdgcn_s_sleep(2);
        }
        issue(s, hf);
      }
    }
    if (blk == 2) TS(8);
    if (blk == 4) TS(9);
    if (blk == 31) TS(10);
    have[0] = blk + 1 < NSEG / 2 && __all((lane & 1) || nf >= s + 2);
    have[1] = blk + 1 < NSEG / 2 && __all(!(lane & 1) || nf >= s + 3);
    __builtin_amdgcn_fence(__ATOMIC_ACQUIRE, "wavefront");
    const int nb[2] = {have[0] ? lane * 16 : OOB, have[1] ? lane * 16 : OOB};
    f32x4 acc[4][3];
#pragma unroll
    for (int i = 0; i < 12; ++i) acc[i / 3][i % 3] = f32x4{0.f, 0.f, 0.f, 0.f};
#pragma unroll
    for (int f = 0; f < 8; ++f) {
      SB();
#pragma unroll
      for (int tt = 0; tt < 4; ++tt)
#pragma unroll
        for (int i = 0; i < 3; ++i) acc[tt][i] = mfma32(wf[f][i], of[tt][f], acc[tt][i]);
      SB();
#pragma unroll
      for (int tt = 0; tt < 4; ++tt) of[tt][f] = ld128(rs, nb[tt >> 1] + ((tt & 1) * 4 + (f & 3)) * 1024, OFF_O + ((s + 2 + (tt >> 1)) * 32 + h0 + (f >> 2)) * 8192);
    }
    SB();
    nf = __hip_atomic_load(fp, RLX_AGENT);
#pragma unroll
    for (int i = 0; i < 12; ++i) red[(w * 12 + i) * 64 + lane] = acc[i / 3][i % 3];
    __syncthreads();
    const int slab = OFF_SLAB + ((blk * 12 + cg) * 4) * 12288;
#pragma unroll
    for (int k = 0; k < 3; ++k) { const int it = tid + 256 * k;
      st128(__builtin_bit_cast(v4u, (red[it] + red[768 + it]) + (red[1536 + it] + red[2304 + it])), rs, it * 16, slab + kg * 12288); }
    publish_add(a.ws, FL_TK + (blk * 12 + cg) * 64, tid == 0);
    if (pend >= 0) combine(pend);
    pend = blk;
  }
  combine(pend);
}

__global__ void __launch_bounds__(256, 1) fused_kernel(Args a) {
  extern __shared__ __attribute__((aligned(16))) unsigned char lds_[];
  LAS unsigned char* lds = (LAS unsigned char*)lds_;
  const int b = blockIdx.x, x = b & 7, r = b >> 3, w = __builtin_amdgcn_readfirstlane(threadIdx.x >> 6);
  if (x < 4 && r < 24) chain_role(a, lds, w, r * 4 + x);
  else {
    const int y = x & 3;
    int role, id;
    if (x >= 4) { if (r < 8) { role = 0; id = 8 * y + r; } else if (r < 20) { role = 2; id = 4 * (r - 8) + y; } else { role = 1; id = 12 * y + r - 20; } }
    else if (r < 28) { role = 1; id = 48 + 4 * y + r - 24; } else { role = 2; id = 4 * (r - 16) + y; }
    if (role == 0) kv_role(a, lds, w, id);
    else if (role == 1) qa_role(a, lds, w, id >> 1, id & 1);
    else bo_role(a, lds, w, id);
  }
}

static int g_d1 = 350, g_r0 = 1150, g_a0 = 3600, g_delta = 460;

extern "C" void kernel_launch(void* const* d_in, const int* in_sizes, int n_in, void* d_out, int out_size, void* d_ws, size_t ws_size,
                              hipStream_t stream) {
  unsigned char* ws = (unsigned char*)d_ws;
  (void)hipMemsetAsync(ws + OFF_FL, 0xFF, FL_BYTES, stream);
  (void)hipFuncSetAttribute((const void*)fused_kernel, hipFuncAttributeMaxDynamicSharedMemorySize, LDS_BYTES);
  Args a{(const u16*)d_in[0], (const u16*)d_in[1], (const u16*)d_in[2], (const u16*)d_in[3], (const u16*)d_in[4], (const u16*)d_in[5],
         (const u16*)d_in[8], (const u16*)d_in[9], (const u16*)d_in[10], (u16*)d_out, ws, g_d1, g_r0, g_a0, g_delta};
  fused_kernel<<<256, 256, LDS_BYTES, stream>>>(a);
}
```
